```python
import torch

def _softmax(x, axis=-1):
    return torch.softmax(x, dim=axis)
def _gelu(x):
    return torch.nn.functional.gelu(x, approximate="tanh")
def _silu(x):
    return torch.nn.functional.silu(x)
def _sigmoid(x):
    return torch.sigmoid(x)
def _layernorm(x, axis=-1, eps=1e-5):
    m = x.mean(dim=axis, keepdim=True)
    v = x.var(dim=axis, keepdim=True, unbiased=False)
    return (x - m) / torch.sqrt(v + eps)
def _scatter_add(tgt, idx, src, axis=0):
    return tgt.clone().index_add_(axis, idx, src)
def _segment_sum(src, seg, n):
    out = torch.zeros((n,) + src.shape[1:], dtype=src.dtype)
    return out.index_add_(0, seg, src)
def _pad(x, pw):
    flat = tuple(v for pair in reversed(pw) for v in pair)
    return torch.nn.functional.pad(x, flat)

kernel_name = 'concat_heads__mha_block_cached__sliding_block_cached__decode'

def setup_inputs():

    _w = (lambda a: (lambda t: t.to(torch.bfloat16) if t.is_floating_point() else t)(torch.as_tensor(a)))
    def _A_setup():
        B, Sq, D = 1, 1, 512
        H, Sk, Dh = 32, 32768, 128
        return {
            "x": _w((lambda *s: torch.randn(*s, dtype=torch.bfloat16) * 0.8)(B, Sq, D)),
            "k_cache": _w((lambda *s: torch.randn(*s, dtype=torch.bfloat16) * 1.6)(B, H, Sk, Dh)),
            "v_cache": _w((lambda *s: torch.randn(*s, dtype=torch.bfloat16) * 0.2)(B, H, Sk, Dh)),
            "wq": _w((lambda *s: torch.randn(*s, dtype=torch.bfloat16) * 1.6)(D, H * Dh) * (D ** -0.5)),
            "wo": _w((lambda *s: torch.randn(*s, dtype=torch.bfloat16) * 0.2)(H * Dh, D) * ((H * Dh) ** -0.5)),
        }
    _A_in = _A_setup()

    def _B_setup():
        B, Sq, D = 1, 1, 512
        Hq, Hkv, Sk, Dh = 32, 8, 32768, 128
        assert Hq % Hkv == 0, (Hq, Hkv)
        return {
            "x": _w((lambda *s: torch.randn(*s, dtype=torch.bfloat16) * 0.8)(B, Sq, D)),
            "k_cache": _w((lambda *s: torch.randn(*s, dtype=torch.bfloat16) * 1.6)(B, Hkv, Sk, Dh)),
            "v_cache": _w((lambda *s: torch.randn(*s, dtype=torch.bfloat16) * 0.2)(B, Hkv, Sk, Dh)),
            "wq": _w((lambda *s: torch.randn(*s, dtype=torch.bfloat16) * 1.6)(D, Hq * Dh) * (D ** -0.5)),
            "wo": _w((lambda *s: torch.randn(*s, dtype=torch.bfloat16) * 0.2)(Hq * Dh, D) * ((Hq * Dh) ** -0.5)),
        }
    _B_in = _B_setup()

    return {
        "A_x": _A_in["x"],
        "A_k_cache": _A_in["k_cache"],
        "A_v_cache": _A_in["v_cache"],
        "A_wq": _A_in["wq"],
        "A_wo": _A_in["wo"],
        "B_x": _B_in["x"],
        "B_k_cache": _B_in["k_cache"],
        "B_v_cache": _B_in["v_cache"],
        "B_wq": _B_in["wq"],
        "B_wo": _B_in["wo"],
    }

def reference(A_x, A_k_cache, A_v_cache, A_wq, A_wo, B_x, B_k_cache, B_v_cache, B_wq, B_wo):
    def _A_ref(x, k_cache, v_cache, wq, wo):
        B, Sq, D = x.shape
        H, Dh = 32, 128
        q = (lambda x, *d: x.permute(*d))((x @ wq).reshape(B, Sq, H, Dh), 0, 2, 1, 3)
        scores = torch.einsum("bhqd,bhkd->bhqk", q, k_cache) * (Dh ** -0.5)
        p = _softmax(scores, axis=-1)
        o = torch.einsum("bhqk,bhkd->bhqd", p, v_cache)
        return (lambda x, *d: x.permute(*d))(o, 0, 2, 1, 3).reshape(B, Sq, H * Dh) @ wo

    def _B_ref(x, k_cache, v_cache, wq, wo):
        B, Sq, D = x.shape
        Hq, Dh = 32, 128
        Hkv, Sk = k_cache.shape[1], k_cache.shape[2]
        W = min(4096, max(256, Sk // 4))
        rep = Hq // Hkv
        q = (lambda x, *d: x.permute(*d))((x @ wq).reshape(B, Sq, Hq, Dh), 0, 2, 1, 3)
        k = (lambda x, n, axis: torch.repeat_interleave(x, n, dim=axis))(k_cache[:, :, Sk - W:, :], rep, axis=1)
        v = (lambda x, n, axis: torch.repeat_interleave(x, n, dim=axis))(v_cache[:, :, Sk - W:, :], rep, axis=1)
        scores = torch.einsum("bhqd,bhkd->bhqk", q, k) * (Dh ** -0.5)
        p = _softmax(scores, axis=-1)
        o = torch.einsum("bhqk,bhkd->bhqd", p, v)
        return (lambda x, *d: x.permute(*d))(o, 0, 2, 1, 3).reshape(B, Sq, Hq * Dh) @ wo

    a = _A_ref(A_x, A_k_cache, A_v_cache, A_wq, A_wo)
    b = _B_ref(B_x, B_k_cache, B_v_cache, B_wq, B_wo)
    return torch.concatenate([a, b], axis=-1)
```

```cpp
#include <hip/hip_runtime.h>
#include <hip/hip_bf16.h>
#include <stdint.h>

typedef unsigned int u32;
typedef u32 u32x4 __attribute__((ext_vector_type(4)));
typedef float f32x4 __attribute__((ext_vector_type(4)));
typedef float f32x2 __attribute__((ext_vector_type(2)));
typedef __bf16 bf16x2_t __attribute__((ext_vector_type(2)));
typedef unsigned short u16;

constexpr int DM = 512;
constexpr int NH = 32;
constexpr int DH = 128;
constexpr int SK = 32768;
constexpr int WIN = 4096;
constexpr int HD = NH * DH;
constexpr int KEYS = 4096;
constexpr int NSPLIT = SK / KEYS;
constexpr int NTASK_A = NH * NSPLIT;
constexpr int NTASK_B = NH;
constexpr int NTASK = NTASK_A + NTASK_B;
constexpr int NW = 4;
constexpr int U = 8;
constexpr int TILE_KEYS = 4 * U;
constexpr int NIT = KEYS / (TILE_KEYS * NW);

constexpr size_t WS_Y = 0;
constexpr size_t WS_ML = (size_t)NTASK * DM;

__device__ __forceinline__ float dot2(u32 a, u32 b, float acc) {
  return __builtin_amdgcn_fdot2_f32_bf16(__builtin_bit_cast(bf16x2_t, a), __builtin_bit_cast(bf16x2_t, b), acc, false);
}
template <int CTRL> __device__ __forceinline__ float dpp(float x) {
  return __builtin_bit_cast(float, __builtin_amdgcn_mov_dpp(__builtin_bit_cast(int, x), CTRL, 0xf, 0xf, true));
}
constexpr int XOR1 = 0xB1, XOR2 = 0x4E, XOR7 = 0x141, XOR8 = 0x128;
constexpr int BCAST0 = 0x150;
__device__ __forceinline__ float bflo(u32 v) { return __builtin_bit_cast(float, v << 16); }
__device__ __forceinline__ float bfhi(u32 v) { return __builtin_bit_cast(float, v & 0xffff0000u); }
__device__ __forceinline__ float xrow_sum(float x) {
  auto s = __builtin_amdgcn_permlane16_swap(__float_as_uint(x), __float_as_uint(x), false, false);
  x = __uint_as_float(s[0]) + __uint_as_float(s[1]);
  auto t = __builtin_amdgcn_permlane32_swap(__float_as_uint(x), __float_as_uint(x), false, false);
  return __uint_as_float(t[0]) + __uint_as_float(t[1]);
}
__device__ __forceinline__ float xrow_max(float x) {
  auto s = __builtin_amdgcn_permlane16_swap(__float_as_uint(x), __float_as_uint(x), false, false);
  x = fmaxf(__uint_as_float(s[0]), __uint_as_float(s[1]));
  auto t = __builtin_amdgcn_permlane32_swap(__float_as_uint(x), __float_as_uint(x), false, false);
  return fmaxf(__uint_as_float(t[0]), __uint_as_float(t[1]));
}
__device__ __forceinline__ u32 pack_bf16(float lo, float hi) {
  u32 r; asm volatile("v_cvt_pk_bf16_f32 %0, %1, %2" : "=v"(r) : "v"(lo), "v"(hi)); return r;
}
typedef __amdgpu_buffer_rsrc_t rsrc_t;
__device__ __forceinline__ rsrc_t mk_rsrc(const void* p, u32 bytes) {
  return __builtin_amdgcn_make_buffer_rsrc(const_cast<void*>(p), 0, bytes, 0x00020000);
}
template <bool NT> __device__ __forceinline__ u32x4 bld16(rsrc_t rs, u32 voff, u32 soff) {
  return __builtin_amdgcn_raw_buffer_load_b128(rs, voff, soff, NT ? 2 : 0);
}
#define SB() __builtin_amdgcn_sched_barrier(0)
__device__ __forceinline__ void fma8(f32x2 (&acc)[4], float s, const u32x4 v) {
  const f32x2 s2 = {s, s};
#pragma unroll
  for (int k = 0; k < 4; ++k) acc[k] = __builtin_elementwise_fma(s2, f32x2{bflo(v[k]), bfhi(v[k])}, acc[k]);
  asm volatile("" : "+v"(acc[0]), "+v"(acc[1]), "+v"(acc[2]), "+v"(acc[3]) :: "memory");
}

template <int N> struct IC { static constexpr int value = N; };
template <int I, int N, typename F> __device__ __forceinline__ void static_for(F&& f) {
  if constexpr (I < N) { f(IC<I>{}); static_for<I + 1, N>(f); }
}

struct State { float m, l; f32x2 acc[4]; };

template <bool NT, bool REISSUE>
__device__ __forceinline__ void tile_step(u32x4 (&kr)[U], u32x4 (&vr)[U], const u32x4 q, State& st,
                                          rsrc_t krs, rsrc_t vrs, u32 voff, u32 soff, int c) {
  constexpr float C2 = 0.08838834764831845f * 1.4426950408889634f;
  float x[U];
#pragma unroll
  for (int t = 0; t < U; ++t) {
    float d = 0.f;
#pragma unroll
    for (int k = 0; k < 4; ++k) d = dot2(kr[t][k], q[k], d);
    asm volatile("" : "+v"(d) :: "memory");
    x[t] = d;
    SB();
    if constexpr (REISSUE) kr[t] = bld16<NT>(krs, voff + (t & 3) * 1024, soff + (t >> 2) * 4096);
    SB();
  }
#pragma unroll
  for (int h = 0; h < 4; ++h) { const float a = x[h] + dpp<XOR8>(x[h]), b = x[h + 4] + dpp<XOR8>(x[h + 4]); x[h] = (c & 8) ? b : a; }
#pragma unroll
  for (int h = 0; h < 2; ++h) { const float a = x[h] + dpp<XOR7>(x[h]), b = x[h + 2] + dpp<XOR7>(x[h + 2]); x[h] = (c & 4) ? b : a; }
  { const float a = x[0] + dpp<XOR2>(x[0]), b = x[1] + dpp<XOR2>(x[1]); x[0] = (c & 2) ? b : a; }
  const float s = (x[0] + dpp<XOR1>(x[0])) * C2;
  float pm = fmaxf(s, dpp<XOR2>(s)); pm = fmaxf(pm, dpp<XOR7>(pm)); pm = fmaxf(pm, dpp<XOR8>(pm));
  const float mn = fmaxf(st.m, pm);
  const float alpha = __builtin_amdgcn_exp2f(st.m - mn);
  const float p = __builtin_amdgcn_exp2f(s - mn);
  st.m = mn;
  st.l = fmaf(st.l, alpha, p);
#pragma unroll
  for (int k = 0; k < 4; ++k) st.acc[k] *= alpha;
  static_for<0, U>([&](auto T) {
    constexpr int t = decltype(T)::value;
    const float pb = dpp<BCAST0 + 2 * t>(p);
    fma8(st.acc, pb, vr[t]);
    SB();
    if constexpr (REISSUE) vr[t] = bld16<NT>(vrs, voff + (t & 3) * 1024, soff + (t >> 2) * 4096);
    SB();
  });
}

template <bool NT>
__device__ __forceinline__ void run_task(const u16* __restrict__ x, const u16* __restrict__ wq, const u16* __restrict__ kp,
                                         const u16* __restrict__ vp, const u16* __restrict__ wo, float* __restrict__ yout,
                                         float* __restrict__ mlout, float* lds) {
  const int tid = threadIdx.x, lane = tid & 63, wave = __builtin_amdgcn_readfirstlane(tid >> 6);
  const int r = lane >> 4, c = lane & 15;
  const float xv0 = bflo(x[128 * wave + 4 * c + r]);
  const float xv1 = bflo(x[128 * wave + 64 + 4 * c + r]);
  u32x4 w[32];
  SB();
  {
    const rsrc_t wrs = mk_rsrc(wq, (DM - 1) * (HD * 2) + DH * 2);
    const u32 wv = r * (HD * 2) + c * 16;
#pragma unroll
    for (int j = 0; j < 32; ++j) { w[j] = bld16<false>(wrs, wv, (128 * wave + 4 * j) * (HD * 2)); SB(); }
  }
  const rsrc_t krs = mk_rsrc(kp, KEYS * 256), vrs = mk_rsrc(vp, KEYS * 256);
  const u32 voff = lane * 16;
  const u32 soff0 = wave * (TILE_KEYS * 256);
  constexpr int STEP = NW * TILE_KEYS * 256;
  u32x4 kr[U], vr[U];
#pragma unroll
  for (int t = 0; t < U; ++t) { kr[t] = bld16<NT>(krs, voff + (t & 3) * 1024, soff0 + (t >> 2) * 4096); SB(); }
#pragma unroll
  for (int t = 0; t < U; ++t) { vr[t] = bld16<NT>(vrs, voff + (t & 3) * 1024, soff0 + (t >> 2) * 4096); SB(); }
  f32x2 qa2[4];
#pragma unroll
  for (int k = 0; k < 4; ++k) qa2[k] = f32x2{0.f, 0.f};
  static_for<0, 32>([&](auto J) {
    constexpr int j = decltype(J)::value;
    const float xb = dpp<BCAST0 + (j & 15)>(j < 16 ? xv0 : xv1);
    fma8(qa2, xb, w[j]);
    SB();
  });
  float qa[8];
#pragma unroll
  for (int k = 0; k < 8; ++k) qa[k] = xrow_sum(qa2[k >> 1][k & 1]);
  if (r == 0) {
    *reinterpret_cast<f32x4*>(lds + wave * 128 + 8 * c) = f32x4{qa[0], qa[1], qa[2], qa[3]};
    *reinterpret_cast<f32x4*>(lds + wave * 128 + 8 * c + 4) = f32x4{qa[4], qa[5], qa[6], qa[7]};
  }
  __syncthreads();
  u32x4 q;
  {
    f32x4 a = *reinterpret_cast<const f32x4*>(lds + 8 * c), b = *reinterpret_cast<const f32x4*>(lds + 8 * c + 4);
#pragma unroll
    for (int ww = 1; ww < NW; ++ww) {
      a += *reinterpret_cast<const f32x4*>(lds + ww * 128 + 8 * c);
      b += *reinterpret_cast<const f32x4*>(lds + ww * 128 + 8 * c + 4);
    }
    q[0] = pack_bf16(a[0], a[1]); q[1] = pack_bf16(a[2], a[3]); q[2] = pack_bf16(b[0], b[1]); q[3] = pack_bf16(b[2], b[3]);
  }
  SB();
  {
    const rsrc_t ors = mk_rsrc(wo, DH * DM * 2);
#pragma unroll
    for (int j = 0; j < 32; ++j) { w[j] = bld16<false>(ors, voff + (j & 3) * 1024, (32 * wave + (j & ~3)) * (DM * 2)); SB(); }
  }
  State st;
  st.m = -INFINITY; st.l = 0.f;
#pragma unroll
  for (int k = 0; k < 4; ++k) st.acc[k] = f32x2{0.f, 0.f};
  tile_step<NT, true>(kr, vr, q, st, krs, vrs, voff, soff0 + STEP, c);
#pragma unroll 1
  for (int i = 1; i < NIT - 1; ++i) tile_step<NT, true>(kr, vr, q, st, krs, vrs, voff, soff0 + (i + 1) * STEP, c);
  tile_step<NT, false>(kr, vr, q, st, krs, vrs, voff, 0, c);
  const float mw = xrow_max(st.m);
  const float sc = __builtin_amdgcn_exp2f(st.m - mw);
  float lw = st.l * sc;
  lw += dpp<XOR2>(lw); lw += dpp<XOR7>(lw); lw += dpp<XOR8>(lw);
  lw = xrow_sum(lw);
  float oa[8];
#pragma unroll
  for (int k = 0; k < 8; ++k) oa[k] = xrow_sum(st.acc[k >> 1][k & 1] * sc);
  float* mo = lds + 512;
  float* mml = lds + 1024;
  if (r == 0) {
    *reinterpret_cast<f32x4*>(mo + wave * 128 + 8 * c) = f32x4{oa[0], oa[1], oa[2], oa[3]};
    *reinterpret_cast<f32x4*>(mo + wave * 128 + 8 * c + 4) = f32x4{oa[4], oa[5], oa[6], oa[7]};
    if (c == 0) { mml[2 * wave] = mw; mml[2 * wave + 1] = lw; }
  }
  __syncthreads();
  float M = mml[0];
#pragma unroll
  for (int ww = 1; ww < NW; ++ww) M = fmaxf(M, mml[2 * ww]);
  float L = 0.f, ov = 0.f;
#pragma unroll
  for (int ww = 0; ww < NW; ++ww) {
    const float f = __builtin_amdgcn_exp2f(mml[2 * ww] - M);
    L = fmaf(f, mml[2 * ww + 1], L);
    ov = fmaf(f, mo[ww * 128 + 32 * wave + (lane & 31)], ov);
  }
  f32x2 y[4];
#pragma unroll
  for (int k = 0; k < 4; ++k) y[k] = f32x2{0.f, 0.f};
  SB();
#pragma unroll
  for (int j = 0; j < 32; ++j) {
    const float oj = __builtin_bit_cast(float, __builtin_amdgcn_readlane(__builtin_bit_cast(int, ov), j));
    fma8(y, oj, w[j]);
    SB();
  }
  float* my = lds + 1536;
  *reinterpret_cast<f32x4*>(my + wave * 512 + 8 * lane) = f32x4{y[0][0], y[0][1], y[1][0], y[1][1]};
  *reinterpret_cast<f32x4*>(my + wave * 512 + 8 * lane + 4) = f32x4{y[2][0], y[2][1], y[3][0], y[3][1]};
  __syncthreads();
  {
    float2 a = *reinterpret_cast<const float2*>(my + 2 * tid);
#pragma unroll
    for (int ww = 1; ww < NW; ++ww) { const float2 b = *reinterpret_cast<const float2*>(my + ww * 512 + 2 * tid); a.x += b.x; a.y += b.y; }
    *reinterpret_cast<float2*>(yout + 2 * tid) = a;
    if (tid == 0) *reinterpret_cast<float2*>(mlout) = make_float2(M, L);
  }
}

__global__ void __launch_bounds__(256, 2)
attn_main(const u16* __restrict__ xa, const u16* __restrict__ ka, const u16* __restrict__ va, const u16* __restrict__ wqa,
          const u16* __restrict__ woa, const u16* __restrict__ xb, const u16* __restrict__ kbp, const u16* __restrict__ vbp,
          const u16* __restrict__ wqb, const u16* __restrict__ wob, float* __restrict__ ws) {
  __shared__ __attribute__((aligned(16))) float lds[1536 + NW * 512];
  const int b = blockIdx.x;
  if (b < NTASK_A) {
    const int xcd = b & 7, idx = b >> 3;
    const int h = xcd * 4 + (idx >> 3), s = idx & 7;
    const int task = h * NSPLIT + s;
    const size_t kvoff = ((size_t)h * SK + (size_t)s * KEYS) * DH;
    run_task<true>(xa, wqa + h * DH, ka + kvoff, va + kvoff, woa + (size_t)h * DH * DM, ws + WS_Y + (size_t)task * DM,
                   ws + WS_ML + 2 * task, lds);
  } else {
    const int g = b & 7, h = g * 4 + ((b - NTASK_A) >> 3);
    const int task = NTASK_A + h;
    const size_t kvoff = ((size_t)g * SK + (SK - WIN)) * DH;
    run_task<false>(xb, wqb + h * DH, kbp + kvoff, vbp + kvoff, wob + (size_t)h * DH * DM, ws + WS_Y + (size_t)task * DM,
                    ws + WS_ML + 2 * task, lds);
  }
}

__global__ void __launch_bounds__(256) attn_final(const float* __restrict__ ws, __hip_bfloat16* __restrict__ out) {
  __shared__ __attribute__((aligned(16))) float red[4][32];
  const int tid = threadIdx.x, hs = tid >> 3, qd = tid & 7, lane = tid & 63, wave = tid >> 6;
  const int b = blockIdx.x;
  f32x4 acc;
  if (b < 16) {
    const float* Y = ws + WS_Y + (size_t)(hs * NSPLIT) * DM + b * 32 + qd * 4;
    const float* ML = ws + WS_ML + 2 * hs * NSPLIT;
    f32x4 y[NSPLIT], ml[NSPLIT / 2];
#pragma unroll
    for (int k = 0; k < NSPLIT / 2; ++k) ml[k] = *reinterpret_cast<const f32x4*>(ML + 4 * k);
#pragma unroll
    for (int k = 0; k < NSPLIT; ++k) y[k] = *reinterpret_cast<const f32x4*>(Y + (size_t)k * DM);
    float M = -INFINITY;
#pragma unroll
    for (int k = 0; k < NSPLIT / 2; ++k) M = fmaxf(M, fmaxf(ml[k][0], ml[k][2]));
    float L = 0.f;
    acc = f32x4{0.f, 0.f, 0.f, 0.f};
#pragma unroll
    for (int k = 0; k < NSPLIT; ++k) {
      const float f = __builtin_amdgcn_exp2f(ml[k >> 1][(k & 1) * 2] - M);
      L = fmaf(f, ml[k >> 1][(k & 1) * 2 + 1], L);
      acc += f * y[k];
    }
    acc *= (1.f / L);
  } else {
    const float* Y = ws + WS_Y + (size_t)(NTASK_A + hs) * DM + (b - 16) * 32 + qd * 4;
    const float L = ws[WS_ML + 2 * (NTASK_A + hs) + 1];
    acc = *reinterpret_cast<const f32x4*>(Y) * (1.f / L);
  }
#pragma unroll
  for (int k = 0; k < 4; ++k) {
    float v = acc[k];
    v += dpp<XOR8>(v);
    v = xrow_sum(v);
    acc[k] = v;
  }
  if (lane < 8) *reinterpret_cast<f32x4*>(&red[wave][4 * lane]) = acc;
  __syncthreads();
  if (tid < 32) out[b * 32 + tid] = __float2bfloat16(red[0][tid] + red[1][tid] + red[2][tid] + red[3][tid]);
}

extern "C" void kernel_launch(void* const* d_in, const int* in_sizes, int n_in, void* d_out, int out_size, void* d_ws,
                              size_t ws_size, hipStream_t stream) {
  hipLaunchKernelGGL(attn_main, dim3(NTASK), dim3(256), 0, stream, (const u16*)d_in[0], (const u16*)d_in[1],
                     (const u16*)d_in[2], (const u16*)d_in[3], (const u16*)d_in[4], (const u16*)d_in[5],
                     (const u16*)d_in[6], (const u16*)d_in[7], (const u16*)d_in[8], (const u16*)d_in[9], (float*)d_ws);
  hipLaunchKernelGGL(attn_final, dim3(32), dim3(256), 0, stream, (const float*)d_ws, (__hip_bfloat16*)d_out);
}
```

```cpp
#include <hip/hip_runtime.h>
#include <hip/hip_bf16.h>
#include <stdint.h>

typedef unsigned int u32;
typedef u32 u32x4 __attribute__((ext_vector_type(4)));
typedef float f32x4 __attribute__((ext_vector_type(4)));
typedef float f32x2 __attribute__((ext_vector_type(2)));
typedef __bf16 bf16x2_t __attribute__((ext_vector_type(2)));
typedef unsigned short u16;

#define UU 4
#define WAUX 2
#define BAUX 2
#define NPRE 3
#define WOS 8
#define NSPL 7

constexpr int DM = 512;
constexpr int NH = 32;
constexpr int DH = 128;
constexpr int SK = 32768;
constexpr int WIN = 4096;
constexpr int HD = NH * DH;
constexpr int NSPLIT = NSPL;
constexpr int NTASK_A = NH * NSPLIT;
constexpr int NTASK_B = NH;
constexpr int NTASK = NTASK_A + NTASK_B;
constexpr int NW = 4;
constexpr int U = UU;
constexpr int TILE_KEYS = 4 * U;
constexpr int WGT_KEYS = TILE_KEYS * NW;
constexpr int NWGT_A = SK / WGT_KEYS;
constexpr int NWGT_B = WIN / WGT_KEYS;

constexpr size_t WS_Y = 0;
constexpr size_t WS_ML = (size_t)NTASK * DM;

__device__ __forceinline__ float dot2(u32 a, u32 b, float acc) {
  return __builtin_amdgcn_fdot2_f32_bf16(__builtin_bit_cast(bf16x2_t, a), __builtin_bit_cast(bf16x2_t, b), acc, false);
}
template <int CTRL> __device__ __forceinline__ float dpp(float x) {
  return __builtin_bit_cast(float, __builtin_amdgcn_mov_dpp(__builtin_bit_cast(int, x), CTRL, 0xf, 0xf, true));
}
constexpr int XOR1 = 0xB1, XOR2 = 0x4E, XOR7 = 0x141, XOR8 = 0x128;
constexpr int BCAST0 = 0x150;
__device__ __forceinline__ float bflo(u32 v) { return __builtin_bit_cast(float, v << 16); }
__device__ __forceinline__ float bfhi(u32 v) { return __builtin_bit_cast(float, v & 0xffff0000u); }
__device__ __forceinline__ float xrow_sum(float x) {
  auto s = __builtin_amdgcn_permlane16_swap(__float_as_uint(x), __float_as_uint(x), false, false);
  x = __uint_as_float(s[0]) + __uint_as_float(s[1]);
  auto t = __builtin_amdgcn_permlane32_swap(__float_as_uint(x), __float_as_uint(x), false, false);
  return __uint_as_float(t[0]) + __uint_as_float(t[1]);
}
__device__ __forceinline__ float xrow_max(float x) {
  auto s = __builtin_amdgcn_permlane16_swap(__float_as_uint(x), __float_as_uint(x), false, false);
  x = fmaxf(__uint_as_float(s[0]), __uint_as_float(s[1]));
  auto t = __builtin_amdgcn_permlane32_swap(__float_as_uint(x), __float_as_uint(x), false, false);
  return fmaxf(__uint_as_float(t[0]), __uint_as_float(t[1]));
}
__device__ __forceinline__ u32 pack_bf16(float lo, float hi) {
  u32 r; asm("v_cvt_pk_bf16_f32 %0, %1, %2" : "=v"(r) : "v"(lo), "v"(hi)); return r;
}
__device__ __forceinline__ float round_bf16(float x) { return bflo(pack_bf16(x, x)); }
typedef __amdgpu_buffer_rsrc_t rsrc_t;
__device__ __forceinline__ rsrc_t mk_rsrc(const void* p, u32 bytes) {
  return __builtin_amdgcn_make_buffer_rsrc(const_cast<void*>(p), 0, bytes, 0x00020000);
}
template <int AUX> __device__ __forceinline__ u32x4 bld16(rsrc_t rs, u32 voff, u32 soff) {
  return __builtin_amdgcn_raw_buffer_load_b128(rs, voff, soff, AUX);
}
#define SB() __builtin_amdgcn_sched_barrier(0)
__device__ __forceinline__ void fma8(f32x2 (&acc)[4], float s, const u32x4 v) {
  const f32x2 s2 = {s, s};
#pragma unroll
  for (int k = 0; k < 4; ++k) acc[k] = __builtin_elementwise_fma(s2, f32x2{bflo(v[k]), bfhi(v[k])}, acc[k]);
  asm volatile("" : "+v"(acc[0]), "+v"(acc[1]), "+v"(acc[2]), "+v"(acc[3]) :: "memory");
}

template <int N> struct IC { static constexpr int value = N; };
template <int I, int N, typename F> __device__ __forceinline__ void static_for(F&& f) {
  if constexpr (I < N) { f(IC<I>{}); static_for<I + 1, N>(f); }
}

struct State { float m, l; f32x2 acc[4]; };

template <typename FK, typename FV>
__device__ __forceinline__ void tile_step(u32x4* kr, u32x4* vr, const u32x4 q, State& st, int c, FK&& postk, FV&& postv) {
  constexpr float SCALE = 0.08838834764831845f, LOG2E = 1.4426950408889634f;
  float x[U];
  static_for<0, U>([&](auto T) {
    constexpr int t = decltype(T)::value;
    float d = 0.f;
#pragma unroll
    for (int k = 0; k < 4; ++k) d = dot2(kr[t][k], q[k], d);
    asm volatile("" : "+v"(d) :: "memory");
    x[t] = d;
    SB();
    postk(T);
    SB();
  });
  float s;
  static_assert(U == 4, "");
#pragma unroll
  for (int h = 0; h < 2; ++h) { const float a = x[h] + dpp<XOR8>(x[h]), b = x[h + 2] + dpp<XOR8>(x[h + 2]); x[h] = (c & 8) ? b : a; }
  { const float a = x[0] + dpp<XOR7>(x[0]), b = x[1] + dpp<XOR7>(x[1]); x[0] = (c & 4) ? b : a; }
  x[0] += dpp<XOR2>(x[0]);
  s = x[0] + dpp<XOR1>(x[0]);
  s = round_bf16(round_bf16(s) * SCALE) * LOG2E;
  float pm = s;
  pm = fmaxf(pm, dpp<XOR7>(pm)); pm = fmaxf(pm, dpp<XOR8>(pm));
  const float mn = fmaxf(st.m, pm);
  const float alpha = __builtin_amdgcn_exp2f(st.m - mn);
  const float p = __builtin_amdgcn_exp2f(s - mn);
  st.m = mn;
  st.l = fmaf(st.l, alpha, p);
#pragma unroll
  for (int k = 0; k < 4; ++k) st.acc[k] *= alpha;
  static_for<0, U>([&](auto T) {
    constexpr int t = decltype(T)::value;
    const float pb = dpp<BCAST0 + (16 / U) * t>(p);
    fma8(st.acc, pb, vr[t]);
    SB();
    postv(T);
    SB();
  });
}

template <int AUX>
__device__ __forceinline__ void run_task(const u16* __restrict__ x, const u16* __restrict__ wq, const u16* __restrict__ kp,
                                         const u16* __restrict__ vp, const u16* __restrict__ wo, float* __restrict__ yout,
                                         float* __restrict__ mlout, int nit, float* lds, unsigned long long* tt) {
  const int tid = threadIdx.x, lane = tid & 63, wave = __builtin_amdgcn_readfirstlane(tid >> 6);
  const int r = lane >> 4, c = lane & 15;
  u32 xr0 = x[128 * wave + 4 * c + r];
  u32 xr1 = x[128 * wave + 64 + 4 * c + r];
  const rsrc_t krs = mk_rsrc(kp, nit * WGT_KEYS * 256), vrs = mk_rsrc(vp, nit * WGT_KEYS * 256);
  const u32 voff = lane * 16;
  const u32 soff0 = wave * (TILE_KEYS * 256);
  constexpr int STEP = WGT_KEYS * 256;
  auto ldkv = [&](int i, int e) { return bld16<AUX>(e < 4 ? krs : vrs, voff, soff0 + i * STEP + (e & 3) * 1024); };
  u32x4 w[32];
  u32x4 pre[8 * NPRE];
  SB();
  {
    const rsrc_t wrs = mk_rsrc(wq, (DM - 1) * (HD * 2) + DH * 2);
    const u32 wv = r * (HD * 2) + c * 16;
    static_for<0, 32>([&](auto J) {
      constexpr int j = decltype(J)::value;
      w[j] = bld16<WAUX>(wrs, wv, (128 * wave + 4 * j) * (HD * 2)); SB();
      constexpr int e0 = (j * 8 * NPRE) / 32, e1 = ((j + 1) * 8 * NPRE) / 32;
      static_for<e0, e1>([&](auto E) { constexpr int e = decltype(E)::value; pre[e] = ldkv(e >> 3, e & 7); SB(); });
    });
  }
  asm volatile("" : "+v"(xr0), "+v"(xr1) :: "memory");
  const float xv0 = bflo(xr0), xv1 = bflo(xr1);
  f32x2 qa2[4];
#pragma unroll
  for (int k = 0; k < 4; ++k) qa2[k] = f32x2{0.f, 0.f};
  static_for<0, 32>([&](auto J) {
    constexpr int j = decltype(J)::value;
    const float xb = dpp<BCAST0 + (j & 15)>(j < 16 ? xv0 : xv1);
    fma8(qa2, xb, w[j]);
    SB();
    w[j] = ldkv(NPRE + (j >> 3), j & 7);
    SB();
  });
  float qa[8];
#pragma unroll
  for (int k = 0; k < 8; ++k) qa[k] = xrow_sum(qa2[k >> 1][k & 1]);
  if (r == 0) {
    *reinterpret_cast<f32x4*>(lds + wave * 128 + 8 * c) = f32x4{qa[0], qa[1], qa[2], qa[3]};
    *reinterpret_cast<f32x4*>(lds + wave * 128 + 8 * c + 4) = f32x4{qa[4], qa[5], qa[6], qa[7]};
  }
  __syncthreads();
  u32x4 q;
  {
    f32x4 a = *reinterpret_cast<const f32x4*>(lds + 8 * c), b = *reinterpret_cast<const f32x4*>(lds + 8 * c + 4);
#pragma unroll
    for (int ww = 1; ww < NW; ++ww) {
      a += *reinterpret_cast<const f32x4*>(lds + ww * 128 + 8 * c);
      b += *reinterpret_cast<const f32x4*>(lds + ww * 128 + 8 * c + 4);
    }
    q[0] = pack_bf16(a[0], a[1]); q[1] = pack_bf16(a[2], a[3]); q[2] = pack_bf16(b[0], b[1]); q[3] = pack_bf16(b[2], b[3]);
  }
  State st;
  st.m = -INFINITY; st.l = 0.f;
#pragma unroll
  for (int k = 0; k < 4; ++k) st.acc[k] = f32x2{0.f, 0.f};
  u32x4* kr = pre; u32x4* vr = pre + 4;
  auto nop = [&](auto) {};
  tile_step(kr, vr, q, st, c, [&](auto T) { kr[T.value] = ldkv(NPRE + 4, T.value); }, [&](auto T) { vr[T.value] = ldkv(NPRE + 4, 4 + T.value); });
  static_for<1, NPRE>([&](auto I) { tile_step(pre + 8 * I.value, pre + 8 * I.value + 4, q, st, c, nop, nop); });
  static_for<0, 4>([&](auto I) { tile_step(w + 8 * I.value, w + 8 * I.value + 4, q, st, c, nop, nop); });
  const rsrc_t ors = mk_rsrc(wo, DH * DM * 2);
  static_for<0, WOS>([&](auto I) {
    constexpr int i = NPRE + 4 + I.value;
    constexpr int per = 32 / WOS;
    tile_step(kr, vr, q, st, c,
              [&](auto T) { kr[T.value] = ldkv(i + 1, T.value);
                            if constexpr (T.value < per) { constexpr int j = I.value * per + T.value; SB(); w[j] = bld16<WAUX>(ors, voff, (32 * wave + j) * (DM * 2)); } },
              [&](auto T) { vr[T.value] = ldkv(i + 1, 4 + T.value);
                            if constexpr (T.value + 4 < per) { constexpr int j = I.value * per + T.value + 4; SB(); w[j] = bld16<WAUX>(ors, voff, (32 * wave + j) * (DM * 2)); } });
  });
#pragma unroll 1
  for (int i = NPRE + 4 + WOS; i < nit - 1; ++i)
    tile_step(kr, vr, q, st, c, [&](auto T) { kr[T.value] = ldkv(i + 1, T.value); }, [&](auto T) { vr[T.value] = ldkv(i + 1, 4 + T.value); });
  tile_step(kr, vr, q, st, c, nop, nop);
  const float mw = xrow_max(st.m);
  const float sc = __builtin_amdgcn_exp2f(st.m - mw);
  float lw = st.l * sc;
  lw += dpp<XOR7>(lw); lw += dpp<XOR8>(lw);
  lw = xrow_sum(lw);
  float oa[8];
#pragma unroll
  for (int k = 0; k < 8; ++k) oa[k] = xrow_sum(st.acc[k >> 1][k & 1] * sc);
  float* mo = lds + 512;
  float* mml = lds + 1024;
  if (r == 0) {
    *reinterpret_cast<f32x4*>(mo + wave * 128 + 8 * c) = f32x4{oa[0], oa[1], oa[2], oa[3]};
    *reinterpret_cast<f32x4*>(mo + wave * 128 + 8 * c + 4) = f32x4{oa[4], oa[5], oa[6], oa[7]};
    if (c == 0) { mml[2 * wave] = mw; mml[2 * wave + 1] = lw; }
  }
  __syncthreads();
  float M = mml[0];
#pragma unroll
  for (int ww = 1; ww < NW; ++ww) M = fmaxf(M, mml[2 * ww]);
  float L = 0.f, ov = 0.f;
#pragma unroll
  for (int ww = 0; ww < NW; ++ww) {
    const float f = __builtin_amdgcn_exp2f(mml[2 * ww] - M);
    L = fmaf(f, mml[2 * ww + 1], L);
    ov = fmaf(f, mo[ww * 128 + 32 * wave + (lane & 31)], ov);
  }
  f32x2 y[4];
#pragma unroll
  for (int k = 0; k < 4; ++k) y[k] = f32x2{0.f, 0.f};
  SB();
#pragma unroll
  for (int j = 0; j < 32; ++j) {
    const float oj = __builtin_bit_cast(float, __builtin_amdgcn_readlane(__builtin_bit_cast(int, ov), j));
    fma8(y, oj, w[j]);
    SB();
  }
  float* my = lds + 1536;
  *reinterpret_cast<f32x4*>(my + wave * 512 + 8 * lane) = f32x4{y[0][0], y[0][1], y[1][0], y[1][1]};
  *reinterpret_cast<f32x4*>(my + wave * 512 + 8 * lane + 4) = f32x4{y[2][0], y[2][1], y[3][0], y[3][1]};
  __syncthreads();
  {
    float2 a = *reinterpret_cast<const float2*>(my + 2 * tid);
#pragma unroll
    for (int ww = 1; ww < NW; ++ww) { const float2 b = *reinterpret_cast<const float2*>(my + ww * 512 + 2 * tid); a.x += b.x; a.y += b.y; }
    *reinterpret_cast<float2*>(yout + 2 * tid) = a;
    if (tid == 0) *reinterpret_cast<float2*>(mlout) = make_float2(M, L);
  }
}

__global__ void __launch_bounds__(256, 1)
attn_main(const u16* __restrict__ xa, const u16* __restrict__ ka, const u16* __restrict__ va, const u16* __restrict__ wqa,
          const u16* __restrict__ woa, const u16* __restrict__ xb, const u16* __restrict__ kbp, const u16* __restrict__ vbp,
          const u16* __restrict__ wqb, const u16* __restrict__ wob, float* __restrict__ ws) {
  __shared__ __attribute__((aligned(16))) float lds[1536 + NW * 512];
  const int b = blockIdx.x;
  unsigned long long tt[2] = {0, 0};
  const u16 *x, *wq, *kp, *vp, *wo; int task, mli, nit;
  if (b < NTASK_A) {
    const int xcd = b & 7, idx = b >> 3;
    const int h = xcd * 4 + idx / NSPLIT, s = idx % NSPLIT;
    task = h * NSPLIT + s; mli = h * 8 + s;
    const int t0_ = (NWGT_A * s) / NSPLIT, t1_ = (NWGT_A * (s + 1)) / NSPLIT;
    const size_t kvoff = ((size_t)h * SK + (size_t)t0_ * WGT_KEYS) * DH;
    x = xa; wq = wqa + h * DH; kp = ka + kvoff; vp = va + kvoff; wo = woa + (size_t)h * DH * DM; nit = t1_ - t0_;
  } else {
    const int g = b & 7, h = g * 4 + ((b - NTASK_A) >> 3);
    task = NTASK_A + h; mli = NH * 8 + h;
    const size_t kvoff = ((size_t)g * SK + (SK - WIN)) * DH;
    x = xb; wq = wqb + h * DH; kp = kbp + kvoff; vp = vbp + kvoff; wo = wob + (size_t)h * DH * DM; nit = NWGT_B;
  }
  run_task<2>(x, wq, kp, vp, wo, ws + WS_Y + (size_t)task * DM, ws + WS_ML + 2 * mli, nit, lds, tt);
}

__global__ void __launch_bounds__(256) attn_final(const float* __restrict__ ws, __hip_bfloat16* __restrict__ out) {
  __shared__ __attribute__((aligned(16))) float red[4][32];
  const int tid = threadIdx.x, hs = tid >> 3, qd = tid & 7, lane = tid & 63, wave = tid >> 6;
  const int b = blockIdx.x;
  f32x4 acc;
  if (b < 16) {
    const float* Y = ws + WS_Y + (size_t)(hs * NSPLIT) * DM + b * 32 + qd * 4;
    const float* ML = ws + WS_ML + 2 * hs * 8;
    f32x4 y[NSPLIT]; f32x2 ml[NSPLIT];
#pragma unroll
    for (int k = 0; k < NSPLIT; ++k) ml[k] = *reinterpret_cast<const f32x2*>(ML + 2 * k);
#pragma unroll
    for (int k = 0; k < NSPLIT; ++k) y[k] = *reinterpret_cast<const f32x4*>(Y + (size_t)k * DM);
    float M = -INFINITY;
#pragma unroll
    for (int k = 0; k < NSPLIT; ++k) M = fmaxf(M, ml[k][0]);
    float L = 0.f;
    acc = f32x4{0.f, 0.f, 0.f, 0.f};
#pragma unroll
    for (int k = 0; k < NSPLIT; ++k) {
      const float f = __builtin_amdgcn_exp2f(ml[k][0] - M);
      L = fmaf(f, ml[k][1], L);
      acc += f * y[k];
    }
    acc *= (1.f / L);
  } else {
    const float* Y = ws + WS_Y + (size_t)(NTASK_A + hs) * DM + (b - 16) * 32 + qd * 4;
    const float L = ws[WS_ML + 2 * (NH * 8 + hs) + 1];
    acc = *reinterpret_cast<const f32x4*>(Y) * (1.f / L);
  }
#pragma unroll
  for (int k = 0; k < 4; ++k) {
    float v = acc[k];
    v += dpp<XOR8>(v);
    v = xrow_sum(v);
    acc[k] = v;
  }
  if (lane < 8) *reinterpret_cast<f32x4*>(&red[wave][4 * lane]) = acc;
  __syncthreads();
  if (tid < 32) out[b * 32 + tid] = __float2bfloat16(red[0][tid] + red[1][tid] + red[2][tid] + red[3][tid]);
}

extern "C" void kernel_launch(void* const* d_in, const int* in_sizes, int n_in, void* d_out, int out_size, void* d_ws,
                              size_t ws_size, hipStream_t stream) {
  hipLaunchKernelGGL(attn_main, dim3(NTASK), dim3(256), 0, stream, (const u16*)d_in[0], (const u16*)d_in[1],
                     (const u16*)d_in[2], (const u16*)d_in[3], (const u16*)d_in[4], (const u16*)d_in[5],
                     (const u16*)d_in[6], (const u16*)d_in[7], (const u16*)d_in[8], (const u16*)d_in[9], (float*)d_ws);
  hipLaunchKernelGGL(attn_final, dim3(32), dim3(256), 0, stream, (const float*)d_ws, (__hip_bfloat16*)d_out);
}
```

```cpp
#include <hip/hip_runtime.h>
#include <hip/hip_bf16.h>
#include <stdint.h>

typedef unsigned int u32;
typedef u32 u32x4 __attribute__((ext_vector_type(4)));
typedef float f32x4 __attribute__((ext_vector_type(4)));
typedef float f32x2 __attribute__((ext_vector_type(2)));
typedef __bf16 bf16x2_t __attribute__((ext_vector_type(2)));
typedef __bf16 bf16x8_t __attribute__((ext_vector_type(8)));
typedef unsigned short u16;

#define NEX 2
#define TAU_A 104.0
#define DUR_B 7200.0
#define LEAD 110
#define PHSPAN 1

constexpr int DM = 512;
constexpr int NH = 32;
constexpr int DH = 128;
constexpr int SK = 32768;
constexpr int WIN = 4096;
constexpr int HD = NH * DH;
constexpr int NSPLIT = 7;
constexpr int NTASK_A = NH * NSPLIT;
constexpr int NTASK_B = NH;
constexpr int NTASK = NTASK_A + NTASK_B;
constexpr int NW = 4;
constexpr int WT = 4096;
constexpr int WGT = WT * NW;
constexpr int NWGT_A = SK * 256 / WGT;
constexpr int NWGT_B = WIN * 256 / WGT;
constexpr int MAXT = (NWGT_A + NSPLIT - 1) / NSPLIT;
constexpr int NB = 2 + NEX + 8;

constexpr size_t WS_Y = 0;
constexpr size_t WS_ML = (size_t)NTASK * DM;

constexpr int L_S = 0;
constexpr int L_Q = NW * MAXT * 256;
constexpr int L_A = L_Q + NW * 512;
constexpr int L_ML = L_A + 16 * 272;
constexpr int L_END = L_ML + NW * 8;

__device__ __forceinline__ float dot2(u32 a, u32 b, float acc) {
  return __builtin_amdgcn_fdot2_f32_bf16(__builtin_bit_cast(bf16x2_t, a), __builtin_bit_cast(bf16x2_t, b), acc, false);
}
template <int CTRL> __device__ __forceinline__ float dpp(float x) {
  return __builtin_bit_cast(float, __builtin_amdgcn_mov_dpp(__builtin_bit_cast(int, x), CTRL, 0xf, 0xf, true));
}
constexpr int XOR1 = 0xB1, XOR2 = 0x4E, XOR7 = 0x141, XOR8 = 0x128;
constexpr int BCAST0 = 0x150;
__device__ __forceinline__ float bflo(u32 v) { return __builtin_bit_cast(float, v << 16); }
__device__ __forceinline__ float bfhi(u32 v) { return __builtin_bit_cast(float, v & 0xffff0000u); }
__device__ __forceinline__ float xrow_sum(float x) {
  auto s = __builtin_amdgcn_permlane16_swap(__float_as_uint(x), __float_as_uint(x), false, false);
  x = __uint_as_float(s[0]) + __uint_as_float(s[1]);
  auto t = __builtin_amdgcn_permlane32_swap(__float_as_uint(x), __float_as_uint(x), false, false);
  return __uint_as_float(t[0]) + __uint_as_float(t[1]);
}
__device__ __forceinline__ float xrow_max(float x) {
  auto s = __builtin_amdgcn_permlane16_swap(__float_as_uint(x), __float_as_uint(x), false, false);
  x = fmaxf(__uint_as_float(s[0]), __uint_as_float(s[1]));
  auto t = __builtin_amdgcn_permlane32_swap(__float_as_uint(x), __float_as_uint(x), false, false);
  return fmaxf(__uint_as_float(t[0]), __uint_as_float(t[1]));
}
__device__ __forceinline__ u32 pack_bf16(float lo, float hi) {
  u32 r; asm("v_cvt_pk_bf16_f32 %0, %1, %2" : "=v"(r) : "v"(lo), "v"(hi)); return r;
}
__device__ __forceinline__ float round_bf16(float x) { return bflo(pack_bf16(x, x)); }
typedef __amdgpu_buffer_rsrc_t rsrc_t;
__device__ __forceinline__ rsrc_t mk_rsrc(const void* p, u32 bytes) {
  return __builtin_amdgcn_make_buffer_rsrc(const_cast<void*>(p), 0, bytes, 0x00020000);
}
__device__ __forceinline__ u32x4 bld16(rsrc_t rs, u32 voff, u32 soff) {
  return __builtin_amdgcn_raw_buffer_load_b128(rs, voff, soff, 2);
}
#define SB() __builtin_amdgcn_sched_barrier(0)
__device__ __forceinline__ void fma8(f32x2 (&acc)[4], float s, const u32x4 v) {
  const f32x2 s2 = {s, s};
#pragma unroll
  for (int k = 0; k < 4; ++k) acc[k] = __builtin_elementwise_fma(s2, f32x2{bflo(v[k]), bfhi(v[k])}, acc[k]);
  asm volatile("" : "+v"(acc[0]), "+v"(acc[1]), "+v"(acc[2]), "+v"(acc[3]) :: "memory");
}
template <int N> struct IC { static constexpr int value = N; };
template <int I, int N, typename F> __device__ __forceinline__ void static_for(F&& f) {
  if constexpr (I < N) { f(IC<I>{}); static_for<I + 1, N>(f); }
}
__device__ __forceinline__ u32 now16() { return (u32)__builtin_amdgcn_s_memrealtime() << 4; }

template <typename F>
__device__ __forceinline__ void kstep(u32x4* kr, const u32x4 q, float& m, float* sdst, int c, F&& post) {
  constexpr float SCALE = 0.08838834764831845f, LOG2E = 1.4426950408889634f;
  float x[4];
  static_for<0, 4>([&](auto T) {
    constexpr int t = decltype(T)::value;
    float d = 0.f;
#pragma unroll
    for (int k = 0; k < 4; ++k) d = dot2(kr[t][k], q[k], d);
    asm volatile("" : "+v"(d) :: "memory");
    x[t] = d;
    SB();
    post(T);
    SB();
  });
#pragma unroll
  for (int h = 0; h < 2; ++h) { const float a = x[h] + dpp<XOR8>(x[h]), b = x[h + 2] + dpp<XOR8>(x[h + 2]); x[h] = (c & 8) ? b : a; }
  { const float a = x[0] + dpp<XOR7>(x[0]), b = x[1] + dpp<XOR7>(x[1]); x[0] = (c & 4) ? b : a; }
  x[0] += dpp<XOR2>(x[0]);
  float s = x[0] + dpp<XOR1>(x[0]);
  s = round_bf16(round_bf16(s) * SCALE) * LOG2E;
  m = fmaxf(m, s);
  *sdst = s;
  SB();
}
template <typename F>
__device__ __forceinline__ void vstep(u32x4* vr, float s, float M, float& l, f32x2 (&acc)[4], F&& post) {
  const float p = __builtin_amdgcn_exp2f(s - M);
  l += p;
  static_for<0, 4>([&](auto T) {
    constexpr int t = decltype(T)::value;
    const float pb = dpp<BCAST0 + 4 * t>(p);
    fma8(acc, pb, vr[t]);
    SB();
    post(T);
    SB();
  });
}

__global__ void __launch_bounds__(256, 1)
attn_main(const u16* __restrict__ xa, const u16* __restrict__ ka, const u16* __restrict__ va, const u16* __restrict__ wqa,
          const u16* __restrict__ woa, const u16* __restrict__ xb, const u16* __restrict__ kbp, const u16* __restrict__ vbp,
          const u16* __restrict__ wqb, const u16* __restrict__ wob, float* __restrict__ ws) {
  __shared__ __attribute__((aligned(16))) char lds[L_END];
  const u32 tstart = now16();
  const int b = blockIdx.x;
  const int tid = threadIdx.x, lane = tid & 63, wave = __builtin_amdgcn_readfirstlane(tid >> 6);
  const int r = lane >> 4, c = lane & 15;
  const u16 *x, *wq, *kp, *vp, *wo; int task, mli, nit; u32 so0, sol, step, kvb, tau, phase;
  if (b < NTASK_A) {
    const int xcd = b & 7, idx = b >> 3;
    const int h = xcd * 4 + idx / NSPLIT, s = idx % NSPLIT;
    task = h * NSPLIT + s; mli = h * 8 + s;
    const size_t kvoff = (size_t)h * SK * DH;
    nit = s < 4 ? 74 : 72; so0 = s * WGT; step = NSPLIT * WGT; kvb = SK * 256; sol = (NWGT_A - 4 + s) * WGT;
    x = xa; wq = wqa + h * DH; kp = ka + kvoff; vp = va + kvoff; wo = woa + (size_t)h * DH * DM;
    constexpr u32 ta74 = (u32)(TAU_A * 8.0 * NWGT_A / (NSPLIT * 74.0) + 0.5), ta72 = (u32)(TAU_A * 8.0 * NWGT_A / (NSPLIT * 72.0) + 0.5);
    tau = nit == 74 ? ta74 : ta72;
    phase = (((s * NW + wave) * 32 + h) * (u32)(TAU_A * 8.0 * PHSPAN)) / (NSPLIT * NW * 32);
  } else {
    const int g = b & 7, h = g * 4 + ((b - NTASK_A) >> 3);
    task = NTASK_A + h; mli = NH * 8 + h;
    const size_t kvoff = ((size_t)g * SK + (SK - WIN)) * DH;
    nit = NWGT_B; so0 = 0; step = WGT; kvb = WIN * 256; sol = (NWGT_B - 1) * WGT;
    x = xb; wq = wqb + h * DH; kp = kbp + kvoff; vp = vbp + kvoff; wo = wob + (size_t)h * DH * DM;
    tau = (u32)(DUR_B * 16.0 / 128.0);
    phase = ((wave * 8 + g) * tau) / 32;
  }
  so0 += wave * WT; sol += wave * WT;
  u32 xr0 = x[128 * wave + 4 * c + r];
  u32 xr1 = x[128 * wave + 64 + 4 * c + r];
  const rsrc_t krs = mk_rsrc(kp, kvb), vrs = mk_rsrc(vp, kvb);
  const u32 voff = lane * 16;
  auto ldk = [&](int n, int t) { return bld16(krs, voff, so0 + n * step + t * 1024); };
  u32x4 w[32];
  u32x4 pre[8 + 4 * NEX];
  SB();
  {
    const rsrc_t wrs = mk_rsrc(wq, (DM - 1) * (HD * 2) + DH * 2);
    const u32 wv = r * (HD * 2) + c * 16;
    static_for<0, 32>([&](auto J) {
      constexpr int j = decltype(J)::value;
      w[j] = bld16(wrs, wv, (128 * wave + 4 * j) * (HD * 2)); SB();
      constexpr int NP = 8 + 4 * NEX, e0 = (j * NP) / 32, e1 = ((j + 1) * NP) / 32;
      static_for<e0, e1>([&](auto E) { constexpr int e = decltype(E)::value; pre[e] = ldk(e >> 2, e & 3); SB(); });
    });
  }
  asm volatile("" : "+v"(xr0), "+v"(xr1) :: "memory");
  const float xv0 = bflo(xr0), xv1 = bflo(xr1);
  f32x2 acc[4];
#pragma unroll
  for (int k = 0; k < 4; ++k) acc[k] = f32x2{0.f, 0.f};
  static_for<0, 32>([&](auto J) {
    constexpr int j = decltype(J)::value;
    const float xbv = dpp<BCAST0 + (j & 15)>(j < 16 ? xv0 : xv1);
    fma8(acc, xbv, w[j]);
    SB();
    w[j] = ldk(2 + NEX + (j >> 2), j & 3);
    SB();
  });
  float* lq = reinterpret_cast<float*>(lds + L_Q);
  {
    float qa[8];
#pragma unroll
    for (int k = 0; k < 8; ++k) qa[k] = xrow_sum(acc[k >> 1][k & 1]);
    if (r == 0) {
      *reinterpret_cast<f32x4*>(lq + wave * 128 + 8 * c) = f32x4{qa[0], qa[1], qa[2], qa[3]};
      *reinterpret_cast<f32x4*>(lq + wave * 128 + 8 * c + 4) = f32x4{qa[4], qa[5], qa[6], qa[7]};
    }
  }
  __syncthreads();
  u32x4 q;
  {
    f32x4 a = *reinterpret_cast<const f32x4*>(lq + 8 * c), bq = *reinterpret_cast<const f32x4*>(lq + 8 * c + 4);
#pragma unroll
    for (int ww = 1; ww < NW; ++ww) {
      a += *reinterpret_cast<const f32x4*>(lq + ww * 128 + 8 * c);
      bq += *reinterpret_cast<const f32x4*>(lq + ww * 128 + 8 * c + 4);
    }
    q[0] = pack_bf16(a[0], a[1]); q[1] = pack_bf16(a[2], a[3]); q[2] = pack_bf16(bq[0], bq[1]); q[3] = pack_bf16(bq[2], bq[3]);
  }
  u32 tnb = tstart + LEAD * 16 + phase + NB * tau;
  auto pace = [&]() {
    u32 t1 = now16();
    for (int guard = 0; (int)(t1 - tnb) < 0 && guard < 2000; ++guard) { __builtin_amdgcn_s_sleep(1); t1 = now16(); }
    tnb += tau;
  };
  float* ls = reinterpret_cast<float*>(lds + L_S) + wave * (MAXT * 64) + lane;
  float m = -INFINITY;
  u32x4* r0 = pre; u32x4* r1 = pre + 4;
  auto nop = [&](auto) {};
  kstep(r0, q, m, ls, c, [&](auto T) { if (T.value == 0) pace(); r0[T.value] = ldk(NB, T.value); });
  kstep(r1, q, m, ls + 64, c, [&](auto T) { if (T.value == 0) pace(); r1[T.value] = ldk(NB + 1, T.value); });
  static_for<0, NEX>([&](auto I) { kstep(pre + 8 + 4 * I.value, q, m, ls + (2 + I.value) * 64, c, nop); });
  static_for<0, 8>([&](auto I) { kstep(w + 4 * I.value, q, m, ls + (2 + NEX + I.value) * 64, c, nop); });
  u32x4 wt[32];
  {
    const rsrc_t ors = mk_rsrc(wo, DH * DM * 2);
    const u32 ov = r * (8 * DM * 2) + c * 16;
    static_for<0, 20>([&](auto I) {
      constexpr int i = decltype(I)::value, n = NB + i;
      u32x4* rr = (i & 1) ? r1 : r0;
      kstep(rr, q, m, ls + n * 64, c, [&](auto T) {
        constexpr int t = decltype(T)::value;
        if (t == 0) pace();
        rr[t] = ldk(n + 2, t);
        if constexpr (i < 16 && (t & 1)) { constexpr int j = 2 * i + (t >> 1); SB(); w[j] = bld16(ors, ov, ((j >> 3) * 32 + (j & 7)) * (DM * 2) + wave * 256); }
      });
      if constexpr (i >= 4 && ((i & 3) == 2 || (i & 3) == 3)) {
        constexpr int kb = (i - 4) >> 2, hf = i & 1;
#pragma unroll
        for (int qd = 2 * hf; qd < 2 * hf + 2; ++qd)
#pragma unroll
          for (int jj = 0; jj < 4; ++jj) {
            wt[8 * kb + 2 * qd][jj] = __builtin_amdgcn_perm(w[8 * kb + 2 * jj + 1][qd], w[8 * kb + 2 * jj][qd], 0x05040100u);
            wt[8 * kb + 2 * qd + 1][jj] = __builtin_amdgcn_perm(w[8 * kb + 2 * jj + 1][qd], w[8 * kb + 2 * jj][qd], 0x07060302u);
          }
        asm volatile("" : "+v"(wt[8 * kb + 4 * hf]), "+v"(wt[8 * kb + 4 * hf + 1]), "+v"(wt[8 * kb + 4 * hf + 2]), "+v"(wt[8 * kb + 4 * hf + 3]) :: "memory");
        SB();
      }
    });
  }
  int n = NB + 20;
#pragma unroll 1
  for (; n + 4 <= nit; n += 2) {
    const u32 o3 = min(so0 + (n + 3) * step, sol);
    kstep(r0, q, m, ls + n * 64, c, [&](auto T) { if (T.value == 0) pace(); r0[T.value] = ldk(n + 2, T.value); });
    kstep(r1, q, m, ls + (n + 1) * 64, c, [&](auto T) { if (T.value == 0) pace(); r1[T.value] = bld16(krs, voff, o3 + T.value * 1024); });
  }
  auto ldv = [&](int i, int t) { return bld16(vrs, voff, so0 + i * step + t * 1024); };
  kstep(r0, q, m, ls + n * 64, c, [&](auto T) { if (T.value == 0) pace(); r0[T.value] = ldv(0, T.value); });
  kstep(r1, q, m, ls + (n + 1) * 64, c, [&](auto T) { if (T.value == 0) pace(); r1[T.value] = ldv(1, T.value); });
  float l = 0.f;
#pragma unroll
  for (int k = 0; k < 4; ++k) acc[k] = f32x2{0.f, 0.f};
  m = fmaxf(m, dpp<XOR7>(m)); m = fmaxf(m, dpp<XOR8>(m)); m = xrow_max(m);
  {
    int i = 0;
    float s0 = ls[i * 64], s1 = ls[(i + 1) * 64];
#pragma unroll 1
    for (; i + 4 <= nit; i += 2) {
      const float s0n = ls[(i + 2) * 64], s1n = ls[(i + 3) * 64];
      const u32 o3 = min(so0 + (i + 3) * step, sol);
      vstep(r0, s0, m, l, acc, [&](auto T) { if (T.value == 0) pace(); r0[T.value] = ldv(i + 2, T.value); });
      vstep(r1, s1, m, l, acc, [&](auto T) { if (T.value == 0) pace(); r1[T.value] = bld16(vrs, voff, o3 + T.value * 1024); });
      s0 = s0n; s1 = s1n;
    }
    vstep(r0, s0, m, l, acc, nop);
    vstep(r1, s1, m, l, acc, nop);
  }
  *reinterpret_cast<u32x4*>(lds + L_A + (4 * wave + r) * 272 + c * 16) =
      u32x4{pack_bf16(acc[0][0], acc[0][1]), pack_bf16(acc[1][0], acc[1][1]), pack_bf16(acc[2][0], acc[2][1]), pack_bf16(acc[3][0], acc[3][1])};
  l += dpp<XOR7>(l); l += dpp<XOR8>(l);
  l = xrow_sum(l);
  if (lane == 0) *reinterpret_cast<f32x2*>(lds + L_ML + wave * 8) = f32x2{m, l};
  __syncthreads();
  u32x4 af[4];
#pragma unroll
  for (int kb = 0; kb < 4; ++kb) af[kb] = *reinterpret_cast<const u32x4*>(lds + L_A + c * 272 + (4 * kb + r) * 16);
  const f32x2 mlw = *reinterpret_cast<const f32x2*>(lds + L_ML + r * 8);
  const float M = xrow_max(mlw[0]);
  const float f = __builtin_amdgcn_exp2f(mlw[0] - M);
  const float L = xrow_sum(f * mlw[1]);
  float z[8];
#pragma unroll
  for (int e = 0; e < 8; ++e) {
    f32x4 d = {0.f, 0.f, 0.f, 0.f};
#pragma unroll
    for (int kb = 0; kb < 4; ++kb)
      d = __builtin_amdgcn_mfma_f32_16x16x32_bf16(__builtin_bit_cast(bf16x8_t, af[kb]), __builtin_bit_cast(bf16x8_t, wt[8 * kb + e]), d, 0, 0, 0);
    z[e] = ((d[0] + d[1]) + (d[2] + d[3])) * f;
  }
#pragma unroll
  for (int e = 0; e < 4; ++e) {
    auto s = __builtin_amdgcn_permlane32_swap(__float_as_uint(z[e]), __float_as_uint(z[e + 4]), false, false);
    z[e] = __uint_as_float(s[0]) + __uint_as_float(s[1]);
  }
#pragma unroll
  for (int e = 0; e < 2; ++e) {
    auto s = __builtin_amdgcn_permlane16_swap(__float_as_uint(z[e]), __float_as_uint(z[e + 2]), false, false);
    z[e] = __uint_as_float(s[0]) + __uint_as_float(s[1]);
  }
  *reinterpret_cast<f32x2*>(ws + WS_Y + (size_t)task * DM + 128 * wave + 8 * c + 2 * r) = f32x2{z[0], z[1]};
  if (tid == 0) *reinterpret_cast<f32x2*>(ws + WS_ML + 2 * mli) = f32x2{M, L};
}

__global__ void __launch_bounds__(256) attn_final(const float* __restrict__ ws, __hip_bfloat16* __restrict__ out) {
  __shared__ __attribute__((aligned(16))) float red[4][32];
  const int tid = threadIdx.x, hs = tid >> 3, qd = tid & 7, lane = tid & 63, wave = tid >> 6;
  const int b = blockIdx.x;
  f32x4 acc;
  if (b < 16) {
    const float* Y = ws + WS_Y + (size_t)(hs * NSPLIT) * DM + b * 32 + qd * 4;
    const float* ML = ws + WS_ML + 2 * hs * 8;
    f32x4 y[NSPLIT]; f32x2 ml[NSPLIT];
#pragma unroll
    for (int k = 0; k < NSPLIT; ++k) ml[k] = *reinterpret_cast<const f32x2*>(ML + 2 * k);
#pragma unroll
    for (int k = 0; k < NSPLIT; ++k) y[k] = *reinterpret_cast<const f32x4*>(Y + (size_t)k * DM);
    float M = -INFINITY;
#pragma unroll
    for (int k = 0; k < NSPLIT; ++k) M = fmaxf(M, ml[k][0]);
    float L = 0.f;
    acc = f32x4{0.f, 0.f, 0.f, 0.f};
#pragma unroll
    for (int k = 0; k < NSPLIT; ++k) {
      const float f = __builtin_amdgcn_exp2f(ml[k][0] - M);
      L = fmaf(f, ml[k][1], L);
      acc += f * y[k];
    }
    acc *= (1.f / L);
  } else {
    const float* Y = ws + WS_Y + (size_t)(NTASK_A + hs) * DM + (b - 16) * 32 + qd * 4;
    const float L = ws[WS_ML + 2 * (NH * 8 + hs) + 1];
    acc = *reinterpret_cast<const f32x4*>(Y) * (1.f / L);
  }
#pragma unroll
  for (int k = 0; k < 4; ++k) {
    float v = acc[k];
    v += dpp<XOR8>(v);
    v = xrow_sum(v);
    acc[k] = v;
  }
  if (lane < 8) *reinterpret_cast<f32x4*>(&red[wave][4 * lane]) = acc;
  __syncthreads();
  if (tid < 32) out[b * 32 + tid] = __float2bfloat16(red[0][tid] + red[1][tid] + red[2][tid] + red[3][tid]);
}

extern "C" void kernel_launch(void* const* d_in, const int* in_sizes, int n_in, void* d_out, int out_size, void* d_ws,
                              size_t ws_size, hipStream_t stream) {
  hipLaunchKernelGGL(attn_main, dim3(NTASK), dim3(256), 0, stream, (const u16*)d_in[0], (const u16*)d_in[1],
                     (const u16*)d_in[2], (const u16*)d_in[3], (const u16*)d_in[4], (const u16*)d_in[5],
                     (const u16*)d_in[6], (const u16*)d_in[7], (const u16*)d_in[8], (const u16*)d_in[9], (float*)d_ws);
  hipLaunchKernelGGL(attn_final, dim3(32), dim3(256), 0, stream, (const float*)d_ws, (__hip_bfloat16*)d_out);
}
```

```cpp
#include <hip/hip_runtime.h>
#include <stdint.h>

typedef unsigned int u32;
typedef u32 u32x4 __attribute__((ext_vector_type(4)));
typedef u32 u32x2 __attribute__((ext_vector_type(2)));
typedef float f32x4 __attribute__((ext_vector_type(4)));
typedef float f32x2 __attribute__((ext_vector_type(2)));
typedef __bf16 bf16x2_t __attribute__((ext_vector_type(2)));
typedef __bf16 bf16x8_t __attribute__((ext_vector_type(8)));
typedef unsigned short u16;

#define TT1 55.4
#define TT2 54.9
#define TT3 54.9
#define TREL 100
#define TQD 110
#define QSL 20
#define FB 1
#define FS 0
#define TFIN 150
#define PD 8
#define VD 4
#define WBS 40
#define SW1 24
#define LEAD 160
#define NR 6
#define TQA 300
#define TQB 1500
#define TPO 20
#define TG 24
#define JIT 4
#define CU 12
#define NCU 124
#define VX 32
#define G1 78
#define G2 86
#define D1 20
#define D2 10
#define WBP 1

constexpr int DM = 512;
constexpr int NH = 32;
constexpr int DH = 128;
constexpr int SK = 32768;
constexpr int WIN = 4096;
constexpr int HD = NH * DH;
constexpr int NWG = 256;
constexpr int NW = 4;
constexpr int WT = 4096;
constexpr int WGT = WT * NW;
constexpr int NT = 64;
constexpr int NL = 4, NB = NL + NR;
constexpr u32 T1 = (u32)(TT1 * 16.0 + 0.5), T2 = (u32)(TT2 * 16.0 + 0.5), T3 = (u32)(TT3 * 16.0 + 0.5);
constexpr u32 T_P = LEAD * 16 + SW1 * T1 + (4 + NT - SW1) * T2;
constexpr u32 T_G1 = T_P + 8 * TG * 16 + (G1 - 4 - NT) * T3, T_G2 = T_G1 + D1 * 16 + (G2 - G1) * T3;
constexpr u32 T_END = T_P + NT * T3 + (8 * TG + D1 + D2) * 16;
static_assert(NR >= 4 && NR <= 6 && (NR & 1) == 0 && (VX & 1) == 0, "");

constexpr u32 WS_E = 0;
constexpr u32 WS_Q = 4096;
constexpr u32 WS_MB = WS_Q + 2 * NWG * 1024;
constexpr u32 WS_ML = WS_MB + NH * 512;
constexpr u32 WS_YB = WS_ML + NH * 128;
constexpr u32 WS_Y = WS_YB + NH * 4096;
constexpr u32 WS_OB = WS_Y + NH * 32768;
constexpr u32 WS_END = WS_OB + NH * 16384;

constexpr int L_W = 0;
constexpr int L_S = 131072;
constexpr int L_A = L_S + (NT + 2) * 256;
constexpr int L_Q = L_A + 16 * 272;
constexpr int L_B = L_Q + 5 * 256;
constexpr int L_M = L_B + 2048;
constexpr int L_F = L_M + 32;
constexpr int L_REC = L_F + 32;
constexpr int RECN = 0;
#define REC(k)
constexpr int L_END = L_REC + RECN;
constexpr int F_Q = L_F, F_K = L_F + 4, F_P = L_F + 8, F_M = L_F + 12, F_Y = L_F + 16;

__device__ __forceinline__ float dot2(u32 a, u32 b, float acc) {
  return __builtin_amdgcn_fdot2_f32_bf16(__builtin_bit_cast(bf16x2_t, a), __builtin_bit_cast(bf16x2_t, b), acc, false);
}
template <int CTRL> __device__ __forceinline__ float dpp(float x) {
  return __builtin_bit_cast(float, __builtin_amdgcn_mov_dpp(__builtin_bit_cast(int, x), CTRL, 0xf, 0xf, true));
}
constexpr int XOR1 = 0xB1, XOR2 = 0x4E, XOR7 = 0x141, XOR8 = 0x128, ROR4 = 0x124;
constexpr int BCAST0 = 0x150;
__device__ __forceinline__ float bflo(u32 v) { return __builtin_bit_cast(float, v << 16); }
__device__ __forceinline__ float bfhi(u32 v) { return __builtin_bit_cast(float, v & 0xffff0000u); }
__device__ __forceinline__ float tr32(float a, float b) {
  auto s = __builtin_amdgcn_permlane32_swap(__float_as_uint(a), __float_as_uint(b), false, false);
  return __uint_as_float(s[0]) + __uint_as_float(s[1]);
}
__device__ __forceinline__ float tr16(float a, float b) {
  auto s = __builtin_amdgcn_permlane16_swap(__float_as_uint(a), __float_as_uint(b), false, false);
  return __uint_as_float(s[0]) + __uint_as_float(s[1]);
}
template <int CTRL> __device__ __forceinline__ float trd(float a, float b, bool hi) {
  const float x = a + dpp<CTRL>(a), y = b + dpp<CTRL>(b);
  return hi ? y : x;
}
__device__ __forceinline__ float xrow_sum(float x) { x = tr16(x, x); return tr32(x, x); }
__device__ __forceinline__ float xrow_max(float x) {
  auto s = __builtin_amdgcn_permlane16_swap(__float_as_uint(x), __float_as_uint(x), false, false);
  x = fmaxf(__uint_as_float(s[0]), __uint_as_float(s[1]));
  auto t = __builtin_amdgcn_permlane32_swap(__float_as_uint(x), __float_as_uint(x), false, false);
  return fmaxf(__uint_as_float(t[0]), __uint_as_float(t[1]));
}
__device__ __forceinline__ u32 pack_bf16(float lo, float hi) {
  u32 r; asm("v_cvt_pk_bf16_f32 %0, %1, %2" : "=v"(r) : "v"(lo), "v"(hi)); return r;
}
__device__ __forceinline__ float round_bf16(float x) { return bflo(pack_bf16(x, x)); }
typedef __amdgpu_buffer_rsrc_t rsrc_t;
__device__ __forceinline__ rsrc_t mk_rsrc(const void* p, u32 bytes) {
  return __builtin_amdgcn_make_buffer_rsrc(const_cast<void*>(p), 0, bytes, 0x00020000);
}
__device__ __forceinline__ u32x4 bld16(rsrc_t rs, u32 voff, u32 soff) {
  return __builtin_amdgcn_raw_buffer_load_b128(rs, voff, soff, 2);
}
template <int AUX>
__device__ __forceinline__ u32x4 gld16(rsrc_t rs, u32 voff, u32 soff) {
  asm volatile("" : "+v"(voff) :: "memory");
  return __builtin_amdgcn_raw_buffer_load_b128(rs, voff, soff, AUX);
}
__device__ __forceinline__ void gst16(rsrc_t rs, u32 voff, u32 soff, float a, float b, u32 tag) {
  const u32x4 d = {__float_as_uint(a), tag, __float_as_uint(b), tag};
  __builtin_amdgcn_raw_buffer_store_b128(d, rs, voff, soff, 16);
  asm volatile("s_nop 3" :: "v"(d) : "memory");
}
template <int N, int A0, int SLP, int ST> __device__ __forceinline__ void gatherv(u32x4* g, rsrc_t rs, u32 voff, u32 soff, u32 tag) {
#pragma unroll
  for (int k = 0; k < N; ++k) g[k] = gld16<A0>(rs, voff, soff + k * ST);
#pragma unroll 1
  for (int guard = 0; guard < 300000; ++guard) {
    u32 pend = 0;
#pragma unroll
    for (int k = 0; k < N; ++k) pend |= (__any(((g[k][1] ^ tag) | (g[k][3] ^ tag)) != 0) ? 1u : 0u) << k;
    if (pend == 0) break;
    if (SLP) __builtin_amdgcn_s_sleep(SLP);
#pragma unroll
    for (int k = 0; k < N; ++k) if ((pend >> k) & 1) g[k] = gld16<16>(rs, voff, soff + k * ST);
  }
}
__device__ __forceinline__ f32x2 gval(const u32x4 g) { return f32x2{__uint_as_float(g[0]), __uint_as_float(g[2])}; }
template <int N, int A0, int SLP> __device__ __forceinline__ f32x2 gather(rsrc_t rs, u32 voff, u32 soff, u32 tag) {
  u32x4 g[N];
  gatherv<N, A0, SLP, 1024>(g, rs, voff, soff, tag);
  f32x2 v = gval(g[0]);
#pragma unroll
  for (int k = 1; k < N; ++k) v += gval(g[k]);
  return v;
}
#define SB() __builtin_amdgcn_sched_barrier(0)
#define MB() asm volatile("" ::: "memory")
__device__ __forceinline__ void fma8(f32x2* acc, float s, const u32x4 v) {
  const f32x2 s2 = {s, s};
#pragma unroll
  for (int k = 0; k < 4; ++k) acc[k] = __builtin_elementwise_fma(s2, f32x2{bflo(v[k]), bfhi(v[k])}, acc[k]);
  asm volatile("" : "+v"(acc[0]), "+v"(acc[1]), "+v"(acc[2]), "+v"(acc[3]) :: "memory");
}
template <int N> struct IC { static constexpr int value = N; };
template <int I, int N, typename F> __device__ __forceinline__ void static_for(F&& f) {
  if constexpr (I < N) { f(IC<I>{}); static_for<I + 1, N>(f); }
}
__device__ __forceinline__ u32 now16() { return (u32)__builtin_amdgcn_s_memrealtime() << 4; }
__device__ __forceinline__ void wait_until(u32 t) {
  u32 t1 = now16();
  for (int guard = 0; (int)(t1 - t) < 0 && guard < 20000; ++guard) { __builtin_amdgcn_s_sleep(1); t1 = now16(); }
}
__device__ __forceinline__ void wg_barrier() { asm volatile("s_waitcnt lgkmcnt(0)\n\ts_barrier" ::: "memory"); }
__device__ __forceinline__ void flag_add(char* p, int lane) {
  asm volatile("s_waitcnt lgkmcnt(0)" ::: "memory");
  if (lane == 0) __hip_atomic_fetch_add(reinterpret_cast<u32*>(p), 1u, __ATOMIC_RELAXED, __HIP_MEMORY_SCOPE_WORKGROUP);
  MB();
}
__device__ __forceinline__ void flag_wait(char* p, u32 cnt) {
  const u32 a = (u32)(size_t)(__attribute__((address_space(3))) char*)p;
#pragma unroll 1
  for (int guard = 0; guard < 3000000; ++guard) {
    u32 v;
    asm volatile("ds_read_b32 %0, %1\n\ts_waitcnt lgkmcnt(0)" : "=v"(v) : "v"(a) : "memory");
    if (__builtin_amdgcn_readfirstlane(v) >= cnt) break;
    __builtin_amdgcn_s_sleep(1);
  }
}

template <typename F>
__device__ __forceinline__ float kstep(u32x4* kr, const u32x4 q, int c, F&& post) {
  float x[4];
  static_for<0, 4>([&](auto T) {
    constexpr int t = decltype(T)::value;
    float d = 0.f;
#pragma unroll
    for (int k = 0; k < 4; ++k) d = dot2(kr[t][k], q[k], d);
    asm volatile("" : "+v"(d) :: "memory");
    x[t] = d;
    SB();
    post(T);
    SB();
  });
  x[0] = trd<XOR8>(x[0], x[2], c & 8); x[1] = trd<XOR8>(x[1], x[3], c & 8);
  x[0] = trd<XOR7>(x[0], x[1], c & 4);
  x[0] += dpp<XOR2>(x[0]);
  x[0] += dpp<XOR1>(x[0]);
  return round_bf16(round_bf16(x[0]) * 0.08838834764831845f) * 1.4426950408889634f;
}
template <typename F>
__device__ __forceinline__ void vstep(u32x4* vr, float p, f32x2* acc, F&& post) {
  static_for<0, 4>([&](auto T) {
    constexpr int t = decltype(T)::value;
    fma8(acc, dpp<BCAST0 + 4 * t>(p), vr[t]);
    SB();
    post(T);
    SB();
  });
}
__device__ __forceinline__ f32x2 rowsum(const f32x2* acc) {
  float z[4];
#pragma unroll
  for (int k = 0; k < 4; ++k) z[k] = tr32(acc[k >> 1][k & 1], acc[2 + (k >> 1)][k & 1]);
  return f32x2{tr16(z[0], z[2]), tr16(z[1], z[3])};
}
__device__ __forceinline__ f32x2 sum8(f32x2 v) {
  v[0] += dpp<XOR8>(v[0]); v[1] += dpp<XOR8>(v[1]);
  return f32x2{xrow_sum(v[0]), xrow_sum(v[1])};
}

__global__ void __launch_bounds__(320, 1)
attn_main(const u16* __restrict__ xa, const u16* __restrict__ ka, const u16* __restrict__ va, const u16* __restrict__ wqa,
          const u16* __restrict__ woa, const u16* __restrict__ xb, const u16* __restrict__ kbp, const u16* __restrict__ vbp,
          const u16* __restrict__ wqb, const u16* __restrict__ wob, float* ws, u32* out) {
  __shared__ __attribute__((aligned(16))) char lds[L_END];
  const u32 tstart = now16();
  asm volatile("" :: "s"(xa), "s"(ka), "s"(va), "s"(wqa), "s"(woa), "s"(xb), "s"(kbp), "s"(vbp), "s"(wqb), "s"(wob), "s"(ws), "s"(out));
  const int b = blockIdx.x;
  const int tid = threadIdx.x, lane = tid & 63, wave = __builtin_amdgcn_readfirstlane(tid >> 6);
  const int r = lane >> 4, c = lane & 15;
  const int g = b & 7, idx = b >> 3, h = g * 4 + (idx >> 3), s = idx & 7;
  const u32 voff = lane * 16;
  const rsrc_t wsr = mk_rsrc(ws, WS_END);
  const u32 lbase = (u32)(size_t)(__attribute__((address_space(3))) char*)lds;
  auto nop = [&](auto) {};
  if (__builtin_expect(wave == NW, 1)) {
    u32 tag;
    u32x4 wb[16];
    auto ldwb = [&]() {
      const rsrc_t ro = mk_rsrc(wob, HD * DM * 2);
#pragma unroll
      for (int j = 0; j < 16; ++j) { if (WBS && j && j % 4 == 0) wait_until(tstart + T_P + (TPO + j / 4 * WBS) * 16); wb[j] = bld16(ro, (lane >> 3) * 1024 + (lane & 7) * 16, (h * 128 + 8 * j) * 1024 + s * 128); SB(); }
    };
    {
      u32 x0 = *reinterpret_cast<const u32*>(xa + 64 * s + 4 * c + (r & 2));
      if (lane < 8) reinterpret_cast<u32*>(lds + L_F)[lane] = 0;
      SB();
      u32x4 w[16];
      const rsrc_t ra = mk_rsrc(wqa, DM * HD * 2);
#pragma unroll
      for (int j = 0; j < 16; ++j) { w[j] = bld16(ra, r * (HD * 2) + c * 16, (64 * s + 4 * j) * (HD * 2) + h * 256); SB(); }
      wg_barrier();
      if (!WBP) ldwb();
      asm volatile("s_load_dword %0, %1, %2" : "=s"(tag) : "s"(ws), "s"(WS_E) : "memory");
      asm volatile("" : "+v"(x0));
      SB();
      const float xv = (r & 1) ? bfhi(x0) : bflo(x0);
      f32x2 acc[4] = {{0.f, 0.f}, {0.f, 0.f}, {0.f, 0.f}, {0.f, 0.f}};
      static_for<0, 16>([&](auto J) { fma8(acc, dpp<BCAST0 + J.value>(xv), w[J.value]); SB(); });
      const f32x2 v = rowsum(acc);
      asm volatile("s_waitcnt lgkmcnt(0)" : "+s"(tag) :: "memory");
      tag += 1u;
      gst16(wsr, c * 64 + r * 16, WS_Q + (h * 8 + s) * 1024, v[0], v[1], tag);
      SB();
      REC(0);
    }
    if (TQD) wait_until(now16() + TQD * 16);
    wait_until(tstart + TQA * 16);
    {
      const f32x2 v = gather<8, 0, QSL>(wsr, voff, WS_Q + h * 8192, tag);
      reinterpret_cast<u32*>(lds + L_Q)[lane] = pack_bf16(v[0], v[1]);
      if (lane == 0) *reinterpret_cast<u32*>(lds + L_M + 24) = tag;
      flag_add(lds + F_Q, lane);
    }
    REC(2);
    flag_wait(lds + F_K, NW);
    {
      const f32x2* pq = reinterpret_cast<const f32x2*>(lds + L_B) + lane;
      const f32x2 v = (pq[0] + pq[64]) + (pq[128] + pq[192]);
      gst16(wsr, c * 64 + r * 16, WS_Q + ((NH + h) * 8 + s) * 1024, v[0], v[1], tag);
    }
    REC(1);
    const u32x4* src = reinterpret_cast<const u32x4*>(lds + L_W) + lane;
    {
      const u32x4 q = *reinterpret_cast<const u32x4*>(lds + L_Q + c * 16);
      float* dst = reinterpret_cast<float*>(lds + L_S) + (lane >> 2);
#pragma unroll 1
      for (int i = 0; i < NW * NL; i += 2) {
        u32x4 ka_[4], kb_[4];
#pragma unroll
        for (int t = 0; t < 4; ++t) { ka_[t] = src[2048 * (i >> 2) + 1024 + 256 * (i & 3) + 64 * t]; kb_[t] = src[2048 * (i >> 2) + 1280 + 256 * (i & 3) + 64 * t]; }
        dst[(i & 3) * 64 + (i >> 2) * 16] = kstep(ka_, q, c, nop);
        dst[(i & 3) * 64 + 64 + (i >> 2) * 16] = kstep(kb_, q, c, nop);
      }
    }
    REC(3);
    wait_until(tstart + TQB * 16);
#pragma unroll 1
    for (int k = 0; k < 4; ++k) {
      const f32x2 v = gather<8, 0, 32>(wsr, voff, WS_Q + (NH + 4 * g + ((k + idx) & 3)) * 8192, tag);
      reinterpret_cast<u32*>(lds + L_Q + 256 + ((k + idx) & 3) * 256)[lane] = pack_bf16(v[0], v[1]);
    }
    REC(4);
    {
      u32x4 q[4], kv[4];
      float mb[4], lb[4];
      f32x2 acc[4][4];
#pragma unroll
      for (int k = 0; k < 4; ++k) {
        q[k] = *reinterpret_cast<const u32x4*>(lds + L_Q + 256 + k * 256 + c * 16);
        mb[k] = -INFINITY; lb[k] = 0.f;
#pragma unroll
        for (int j = 0; j < 4; ++j) acc[k][j] = f32x2{0.f, 0.f};
      }
      float* sb = reinterpret_cast<float*>(lds + L_B) + (lane >> 2);
#pragma unroll 1
      for (int i = 0; i < 8; ++i) {
#pragma unroll
        for (int t = 0; t < 4; ++t) kv[t] = src[2048 * (i >> 1) + 256 * (i & 1) + 64 * t];
#pragma unroll
        for (int k = 0; k < 4; ++k) { const float sv = kstep(kv, q[k], c, nop); mb[k] = fmaxf(mb[k], sv); sb[i * 64 + k * 16] = sv; }
      }
#pragma unroll
      for (int k = 0; k < 4; ++k) { mb[k] = fmaxf(mb[k], dpp<XOR7>(mb[k])); mb[k] = fmaxf(mb[k], dpp<XOR8>(mb[k])); mb[k] = xrow_max(mb[k]); }
#pragma unroll 1
      for (int i = 0; i < 8; ++i) {
#pragma unroll
        for (int t = 0; t < 4; ++t) kv[t] = src[2048 * (i >> 1) + 256 * (i & 1) + 512 + 64 * t];
#pragma unroll
        for (int k = 0; k < 4; ++k) { const float p = __builtin_amdgcn_exp2f(sb[i * 64 + k * 16] - mb[k]); lb[k] += p; vstep(kv, p, acc[k], nop); }
      }
      flag_add(lds + F_P, lane);
      REC(5);
      f32x2 v[4];
#pragma unroll
      for (int k = 0; k < 4; ++k) {
        v[k] = rowsum(acc[k]);
        lb[k] += dpp<XOR7>(lb[k]); lb[k] += dpp<XOR8>(lb[k]); lb[k] = xrow_sum(lb[k]);
      }
      const float mm = r & 2 ? (r & 1 ? mb[3] : mb[2]) : (r & 1 ? mb[1] : mb[0]), ll = r & 2 ? (r & 1 ? lb[3] : lb[2]) : (r & 1 ? lb[1] : lb[0]);
      wait_until(tstart + T_P + TPO * 16);
#pragma unroll
      for (int k = 0; k < 4; k += 2) {
        auto sw = __builtin_amdgcn_permlane32_swap(pack_bf16(v[k][0], v[k][1]), pack_bf16(v[k + 1][0], v[k + 1][1]), false, false);
        gst16(wsr, (lane >> 5) * 16384 + (lane & 31) * 16, WS_OB + (4 * g + k) * 16384 + idx * 512, __uint_as_float(sw[0]), __uint_as_float(sw[1]), tag);
      }
      if (c == 0) gst16(wsr, r * 512, WS_MB + g * 2048 + idx * 16, mm, ll, tag);
      SB();
      if (WBP) ldwb();
    }
    {
      flag_wait(lds + F_M, NW);
      const f32x4 mw = *reinterpret_cast<const f32x4*>(lds + L_M);
      float M = fmaxf(fmaxf(mw[0], mw[1]), fmaxf(mw[2], mw[3])), L = 0.f;
#pragma unroll 1
      for (int n = 0; n < NT; n += 4) {
        const f32x4 sv = *reinterpret_cast<const f32x4*>(lds + L_S + n * 256 + voff);
        L += (__builtin_amdgcn_exp2f(sv[0] - M) + __builtin_amdgcn_exp2f(sv[1] - M)) + (__builtin_amdgcn_exp2f(sv[2] - M) + __builtin_amdgcn_exp2f(sv[3] - M));
      }
      L += dpp<XOR1>(L); L += dpp<XOR2>(L); L += dpp<XOR7>(L); L += dpp<XOR8>(L); L = xrow_sum(L);
      REC(6);
      wait_until(tstart + T_G1);
      if (lane == 0) gst16(wsr, 0, WS_ML + h * 128 + s * 16, M, L, tag);
    }
    f32x2 yb;
    {
      u32x4 go[17];
      go[16] = gld16<0>(wsr, (lane & 31) * 16, WS_MB + h * 512);
      gatherv<16, 0, 64, 1024>(go, wsr, voff, WS_OB + h * 16384, tag);
      if (__any(((go[16][1] ^ tag) | (go[16][3] ^ tag)) != 0)) gatherv<1, 16, 64, 0>(go + 16, wsr, (lane & 31) * 16, WS_MB + h * 512, tag);
      const f32x2 ml = gval(go[16]);
      float M = fmaxf(ml[0], dpp<XOR1>(ml[0])); M = fmaxf(M, dpp<XOR2>(M)); M = fmaxf(M, dpp<XOR7>(M)); M = xrow_max(fmaxf(M, dpp<XOR8>(M)));
      const float f = __builtin_amdgcn_exp2f(ml[0] - M);
      float L = f * ml[1];
      L += dpp<XOR1>(L); L += dpp<XOR2>(L); L += dpp<XOR7>(L); L += dpp<XOR8>(L); L = tr16(L, L);
      float* lf = reinterpret_cast<float*>(lds + L_B);
      lf[128 + lane] = f;
      MB();
      f32x2 o0 = {0.f, 0.f}, o1 = {0.f, 0.f};
#pragma unroll
      for (int j = 0; j < 16; ++j) {
        const float w = lf[128 + 2 * j + (lane >> 5)];
        o0 += f32x2{w, w} * f32x2{bflo(go[j][0]), bfhi(go[j][0])}; o1 += f32x2{w, w} * f32x2{bflo(go[j][2]), bfhi(go[j][2])};
      }
      L = 1.f / L;
      *reinterpret_cast<f32x2*>(lf + 8 * c + 2 * (r & 1) + 4 * (r >> 1)) = f32x2{round_bf16(tr32(o0[0], o1[0]) * L), round_bf16(tr32(o0[1], o1[1]) * L)};
      MB();
      f32x2 acc[4] = {{0.f, 0.f}, {0.f, 0.f}, {0.f, 0.f}, {0.f, 0.f}};
#pragma unroll
      for (int j = 0; j < 16; ++j) fma8(acc, lf[8 * j + (lane >> 3)], wb[j]);
      yb = rowsum(acc);
      yb[0] += dpp<XOR8>(yb[0]); yb[1] += dpp<XOR8>(yb[1]);
    }
    REC(7);
    wait_until(tstart + T_G2);
    if (c < 8) gst16(wsr, (c >> 1) * 4096 + (c & 1) * 64 + r * 16, WS_YB + s * 16384 + h * 128, yb[0], yb[1], tag);
    {
      const f32x2 ml = gather<1, 0, 64>(wsr, (lane & 7) * 16, WS_ML + h * 128, tag);
      float M = fmaxf(ml[0], dpp<XOR1>(ml[0])); M = fmaxf(M, dpp<XOR2>(M)); M = fmaxf(M, dpp<XOR7>(M));
      float L = __builtin_amdgcn_exp2f(ml[0] - M) * ml[1];
      L += dpp<XOR1>(L); L += dpp<XOR2>(L); L += dpp<XOR7>(L);
      if (lane == 0) *reinterpret_cast<f32x2*>(lds + L_M + 16) = f32x2{M, 1.f / L};
    }
    REC(8);
    wg_barrier();
    if (s == FB) {
      const f32x2 v = sum8(gather<4, 16, 16>(wsr, voff, WS_YB + h * 4096, tag));
      if (lane < 8) out[DM / 2 + h * 8 + lane] = pack_bf16(v[0], v[1]);
    }
    REC(9);
    return;
  }
  const rsrc_t krs = mk_rsrc(ka + (size_t)h * SK * DH, SK * 256), vrs = mk_rsrc(va + (size_t)h * SK * DH, SK * 256);
  const u32 so0 = s * WGT + wave * WT;
  constexpr u32 step = 8 * WGT;
  u32 tau = T1;
  u32 tnb = tstart + LEAD * 16 + ((((s * NW + wave) * 32 + h) * T1) >> 10);
  int slot = 0;
  u32 slip = 0;
  auto pace = [&]() {
    const u32 t = now16();
    const u32 tw = tnb;
    tau = slot == SW1 ? T2 : slot == NT + 4 ? T3 : tau;
    const int ns = slot < NCU && (int)(t - tstart) < (int)(T_END - TREL * 16) ? max((int)slip + max((int)(t - tnb) - JIT * 16, 0) - CU, 0) : 0;
    tnb += tau + ns - slip + (slot == G1 - 1 ? D1 * 16 : slot == G2 - 1 ? D2 * 16 : 0);
    slip = ns;
    ++slot;
    if ((int)(t - tw) < 0) wait_until(tw);
  };
  const size_t bo = ((size_t)g * SK + (SK - WIN)) * DH;
  u32 x1 = *reinterpret_cast<const u32*>(xb + 64 * s + 16 * wave + 4 * (c & 3) + (r & 2));
  u32x4 wq[4];
#pragma unroll
  for (int j = 0; j < 4; ++j) { wq[j] = bld16(mk_rsrc(wqb, DM * HD * 2), r * (HD * 2) + c * 16, (64 * s + 16 * wave + 4 * j) * (HD * 2) + h * 256); SB(); }
  wg_barrier();
  {
    const rsrc_t bk = mk_rsrc(kbp + bo, WIN * 256), bv = mk_rsrc(vbp + bo, WIN * 256);
#pragma unroll 1
    for (int n = 0; n < 4 + NL; ++n) {
      const rsrc_t rs = n < 2 ? bk : n < 4 ? bv : krs;
      const u32 o = n < 4 ? (idx * 8 + (n & 1) * 4 + wave) * WT : so0 + (n - 4) * step;
      pace();
      asm volatile("s_mov_b32 m0, %0\n\ts_nop 0\n\t"
                   "buffer_load_dwordx4 %1, %2, %3 offen nt lds\n\t"
                   "buffer_load_dwordx4 %1, %2, %3 offen offset:1024 nt lds\n\t"
                   "buffer_load_dwordx4 %1, %2, %3 offen offset:2048 nt lds\n\t"
                   "buffer_load_dwordx4 %1, %2, %3 offen offset:3072 nt lds"
                   :: "s"(lbase + L_W + (wave * 8 + n) * WT), "v"(voff), "s"(rs), "s"(o) : "m0", "memory");
    }
  }
  u32x4 pre[4 * NR];
  static_for<NL, NB>([&](auto N) {
    pace(); SB();
#pragma unroll
    for (int t = 0; t < 4; ++t) { pre[4 * (N.value - NL) + t] = bld16(krs, voff + t * 1024, so0 + N.value * step); SB(); }
    if constexpr (N.value == NL + 2) {
      asm volatile("s_waitcnt vmcnt(12)" : "+v"(x1) :: "memory");
      const float xv = (r & 1) ? bfhi(x1) : bflo(x1);
      f32x2 acc[4] = {{0.f, 0.f}, {0.f, 0.f}, {0.f, 0.f}, {0.f, 0.f}};
      static_for<0, 4>([&](auto J) { fma8(acc, dpp<BCAST0 + J.value>(xv), wq[J.value]); SB(); });
      reinterpret_cast<f32x2*>(lds + L_B)[wave * 64 + lane] = rowsum(acc);
      flag_add(lds + F_K, lane); SB();
    }
  });
  flag_wait(lds + F_Q, 1);
  __builtin_amdgcn_s_setprio(3);
  REC(155);
  const u32x4 q = *reinterpret_cast<const u32x4*>(lds + L_Q + c * 16);
  float* ls = reinterpret_cast<float*>(lds + L_S) + wave * 16 + (lane >> 2);
  float m = -INFINITY;
  auto kst = [&](u32x4* kr, int n, auto post) { const float sv = kstep(kr, q, c, post); m = fmaxf(m, sv); ls[n * 64] = sv; SB(); };
  auto kld = [&](auto J, int n, rsrc_t rs, u32 o) {
    u32x4* rr = pre + 4 * J.value;
    kst(rr, n, [&](auto T) { if (T.value == 0) pace(); rr[T.value] = bld16(rs, voff + T.value * 1024, o); });
  };
  static_for<0, 2>([&](auto J) {
    kld(J, NL + J.value, krs, so0 + (NB + J.value) * step);
    static_for<2 * J.value, 2 * J.value + 2>([&](auto K) { if constexpr (K.value < NR - 2) kst(pre + 4 * (2 + K.value), NL + 2 + K.value, nop); });
  });
#pragma unroll 1
  for (int n = NB; n < NT - 2; n += 2) static_for<0, 2>([&](auto J) { kld(J, n + J.value, krs, so0 + (n + 2 + J.value) * step); });
  u32x4 ex[8];
  {
    kst(pre, NT - 2, nop);
    flag_wait(lds + F_P, 1);
    const rsrc_t rs = mk_rsrc(woa + (size_t)h * DH * DM, DH * DM * 2);
    const u32 wv = r * 1024 + c * 16;
#pragma unroll 1
    for (int j = 0; j < 8; ++j) {
      const int i = (j + s) & 7;
      wait_until(tnb); tnb += TG * 16;
      asm volatile("s_mov_b32 m0, %0\n\ts_nop 0\n\t"
                   "buffer_load_dwordx4 %1, %5, %6 offen lds\n\t"
                   "buffer_load_dwordx4 %2, %5, %6 offen offset:1024 lds\n\t"
                   "buffer_load_dwordx4 %3, %5, %6 offen offset:2048 lds\n\t"
                   "buffer_load_dwordx4 %4, %5, %6 offen offset:3072 lds"
                   :: "s"(lbase + L_W + (wave * 8 + i) * WT), "v"(wv), "v"(wv + 3072), "v"(wv + 6144), "v"(wv + 9216),
                      "s"(rs), "s"(i * 16384 + wave * 256) : "m0", "memory");
    }
    static_for<0, VD>([&](auto J) {
      pace(); SB();
#pragma unroll
      for (int t = 0; t < 4; ++t) { (J.value < 2 ? pre[8 * J.value + t] : ex[4 * (J.value - 2) + t]) = bld16(vrs, voff + t * 1024, so0 + J.value * step); SB(); }
    });
    kst(pre + 4, NT - 1, nop);
  }
#pragma unroll
  for (int k = 0; k < NL; ++k) m = fmaxf(m, ls[k * 64]);
  m = fmaxf(m, dpp<XOR7>(m)); m = fmaxf(m, dpp<XOR8>(m)); m = xrow_max(m);
  if (lane == 0) reinterpret_cast<float*>(lds + L_M)[wave] = m;
  flag_add(lds + F_M, lane);
  f32x2 acc[4] = {{0.f, 0.f}, {0.f, 0.f}, {0.f, 0.f}, {0.f, 0.f}};
  float sc[2] = {ls[0], ls[64]};
  auto vgrp = [&](int i, auto hook) {
    static_for<0, 2>([&](auto J) {
      u32x4* rr = pre + 8 * J.value;
      const float p = __builtin_amdgcn_exp2f(sc[J.value] - m);
      sc[J.value] = ls[(i + 2 + J.value) * 64];
      vstep(rr, p, acc, [&](auto T) { if (T.value == 0) pace(); rr[T.value] = bld16(vrs, voff + T.value * 1024, so0 + (i + 2 + J.value) * step); });
      hook(J);
    });
  };
  if constexpr (VD == 4) {
    vgrp(2, nop);
    static_for<0, 2>([&](auto J) { vstep(ex + 4 * J.value, __builtin_amdgcn_exp2f(ls[(2 + J.value) * 64] - m), acc, nop); });
  }
#pragma unroll 1
  for (int i = VD == 4 ? 4 : 0; i < VX; i += 2) vgrp(i, nop);
  u32x4 wt[32];
  static_for<0, 4>([&](auto KB) {
    constexpr int kb = KB.value;
    u32x4 w[8];
    vgrp(VX + 2 * kb, [&](auto J) {
      if constexpr (J.value == 0) {
#pragma unroll
        for (int j = 0; j < 8; ++j) w[j] = *reinterpret_cast<const u32x4*>(lds + L_W + wave * 32768 + (32 * kb + 8 * r + j) * 256 + c * 16);
      } else {
#pragma unroll
        for (int qd = 0; qd < 4; ++qd)
#pragma unroll
          for (int jj = 0; jj < 4; ++jj) {
            wt[8 * kb + 2 * qd][jj] = __builtin_amdgcn_perm(w[2 * jj + 1][qd], w[2 * jj][qd], 0x05040100u);
            wt[8 * kb + 2 * qd + 1][jj] = __builtin_amdgcn_perm(w[2 * jj + 1][qd], w[2 * jj][qd], 0x07060302u);
          }
#pragma unroll
        for (int k = 0; k < 8; k += 4)
          asm volatile("" : "+v"(wt[8 * kb + k]), "+v"(wt[8 * kb + k + 1]), "+v"(wt[8 * kb + k + 2]), "+v"(wt[8 * kb + k + 3]) :: "memory");
      }
      SB();
    });
  });
#pragma unroll 1
  for (int i = VX + 8; i < NT - 2; i += 2) vgrp(i, nop);
  const u32 tag = *reinterpret_cast<const u32*>(lds + L_M + 24);
  static_for<0, 2>([&](auto J) { vstep(pre + 8 * J.value, __builtin_amdgcn_exp2f(sc[J.value] - m), acc, nop); });
  *reinterpret_cast<u32x4*>(lds + L_A + (4 * wave + r) * 272 + c * 16) =
      u32x4{pack_bf16(acc[0][0], acc[0][1]), pack_bf16(acc[1][0], acc[1][1]), pack_bf16(acc[2][0], acc[2][1]), pack_bf16(acc[3][0], acc[3][1])};
  REC(156);
  wg_barrier();
  {
    u32x4 af[4];
#pragma unroll
    for (int kb = 0; kb < 4; ++kb) af[kb] = *reinterpret_cast<const u32x4*>(lds + L_A + c * 272 + (4 * kb + r) * 16);
    const f32x2 gl = *reinterpret_cast<const f32x2*>(lds + L_M + 16);
    const float f = __builtin_amdgcn_exp2f(reinterpret_cast<const float*>(lds + L_M)[r] - gl[0]) * gl[1];
    float z[8];
#pragma unroll
    for (int e = 0; e < 8; ++e) {
      f32x4 d = {0.f, 0.f, 0.f, 0.f};
#pragma unroll
      for (int kb = 0; kb < 4; ++kb)
        d = __builtin_amdgcn_mfma_f32_16x16x32_bf16(__builtin_bit_cast(bf16x8_t, af[kb]), __builtin_bit_cast(bf16x8_t, wt[8 * kb + e]), d, 0, 0, 0);
      z[e] = ((d[0] + d[1]) + (d[2] + d[3])) * f;
    }
#pragma unroll
    for (int e = 0; e < 4; ++e) z[e] = tr32(z[e], z[e + 4]);
#pragma unroll
    for (int e = 0; e < 2; ++e) z[e] = tr16(z[e], z[e + 2]);
    gst16(wsr, (c >> 1) * 32768 + (c & 1) * 64 + r * 16, WS_Y + wave * (8 * 32768) + (h * 8 + s) * 128, z[0], z[1], tag);
  }
  REC(157);
  if (s == FS) {
    constexpr u32 BIG = 0x20000000u;
    u32 off[8];
    f32x2 val[8];
    u32x4 gy[3][8];
#pragma unroll
    for (int k = 0; k < 8; ++k) { off[k] = voff + k * 1024; val[k] = f32x2{0.f, 0.f}; gy[0][k] = gy[1][k] = gy[2][k] = u32x4{0u, 0u, 0u, 0u}; }
    const u32 base = WS_Y + h * 32768 + wave * 8192;
    auto issue = [&](u32x4* gg) {
#pragma unroll
      for (int k = 0; k < 8; k += 4)
        asm volatile("buffer_load_dwordx4 %0, %4, %8, %9 offen sc1\n\tbuffer_load_dwordx4 %1, %5, %8, %9 offen sc1\n\t"
                     "buffer_load_dwordx4 %2, %6, %8, %9 offen sc1\n\tbuffer_load_dwordx4 %3, %7, %8, %9 offen sc1"
                     : "+v"(gg[k]), "+v"(gg[k + 1]), "+v"(gg[k + 2]), "+v"(gg[k + 3])
                     : "v"(off[k]), "v"(off[k + 1]), "v"(off[k + 2]), "v"(off[k + 3]), "s"(wsr), "s"(base) : "memory");
    };
    auto look = [&](u32x4* gg) -> bool {
      asm volatile("s_waitcnt vmcnt(16)" : "+v"(gg[0]), "+v"(gg[1]), "+v"(gg[2]), "+v"(gg[3]), "+v"(gg[4]), "+v"(gg[5]), "+v"(gg[6]), "+v"(gg[7]) :: "memory");
      u32 all = BIG;
#pragma unroll
      for (int k = 0; k < 8; ++k) {
        const bool ok = gg[k][1] == tag && gg[k][3] == tag && off[k] != BIG;
        val[k] = ok ? gval(gg[k]) : val[k];
        off[k] = ok ? BIG : off[k];
        all &= off[k];
      }
      if (!__any(all != BIG)) return true;
      issue(gg);
      return false;
    };
    wait_until(tstart + T_END + TFIN * 16);
#pragma unroll
    for (int j = 0; j < 3; ++j) { issue(gy[j]); if (j < 2) __builtin_amdgcn_s_sleep(PD); }
#pragma unroll 1
    for (int guard = 0; guard < 200000; ++guard) {
      if (look(gy[0])) break;
      if (look(gy[1])) break;
      if (look(gy[2])) break;
    }
    f32x2 v = sum8(((val[0] + val[1]) + (val[2] + val[3])) + ((val[4] + val[5]) + (val[6] + val[7])));
    f32x2* pf = reinterpret_cast<f32x2*>(lds + L_B) + (lane & 7);
    if (lane < 8) pf[wave * 8] = v;
    asm volatile("s_waitcnt lgkmcnt(0)" ::: "memory");
    u32 cnt = 0;
    if (lane == 0) cnt = __hip_atomic_fetch_add(reinterpret_cast<u32*>(lds + F_Y), 1u, __ATOMIC_RELAXED, __HIP_MEMORY_SCOPE_WORKGROUP);
    MB();
    if (__builtin_amdgcn_readfirstlane(cnt) == NW - 1) {
      v = (pf[0] + pf[8]) + (pf[16] + pf[24]);
      if (lane < 8) out[h * 8 + lane] = pack_bf16(v[0], v[1]);
      if (h == 0 && lane == 0) __builtin_amdgcn_raw_buffer_store_b32(tag, wsr, 0, WS_E, 16);
    }
#pragma unroll
    for (int j = 0; j < 6; ++j) {
      const u32x4* gg = gy[j >> 1] + 4 * (j & 1);
      asm volatile("s_waitcnt vmcnt(0)" :: "v"(gg[0]), "v"(gg[1]), "v"(gg[2]), "v"(gg[3]) : "memory");
    }
  }
}

extern "C" void kernel_launch(void* const* d_in, const int* in_sizes, int n_in, void* d_out, int out_size, void* d_ws,
                              size_t ws_size, hipStream_t stream) {
  hipLaunchKernelGGL(attn_main, dim3(NWG), dim3(320), 0, stream, (const u16*)d_in[0], (const u16*)d_in[1],
                     (const u16*)d_in[2], (const u16*)d_in[3], (const u16*)d_in[4], (const u16*)d_in[5],
                     (const u16*)d_in[6], (const u16*)d_in[7], (const u16*)d_in[8], (const u16*)d_in[9], (float*)d_ws,
                     (u32*)d_out);
}
```
